```python
import torch

def _softmax(x, axis=-1):
    return torch.softmax(x, dim=axis)
def _gelu(x):
    return torch.nn.functional.gelu(x, approximate="tanh")
def _silu(x):
    return torch.nn.functional.silu(x)
def _sigmoid(x):
    return torch.sigmoid(x)
def _layernorm(x, axis=-1, eps=1e-5):
    m = x.mean(dim=axis, keepdim=True)
    v = x.var(dim=axis, keepdim=True, unbiased=False)
    return (x - m) / torch.sqrt(v + eps)
def _scatter_add(tgt, idx, src, axis=0):
    return tgt.clone().index_add_(axis, idx, src)
def _segment_sum(src, seg, n):
    out = torch.zeros((n,) + src.shape[1:], dtype=src.dtype)
    return out.index_add_(0, seg, src)
def _pad(x, pw):
    flat = tuple(v for pair in reversed(pw) for v in pair)
    return torch.nn.functional.pad(x, flat)

kernel_name = 'recurrent_scan__spectral_gfnet__glu_gated_geglu__prompt'

def setup_inputs():

    _w = (lambda a: (lambda t: t.to(torch.bfloat16) if t.is_floating_point() else t)(torch.as_tensor(a)))
    def _A_setup():
        B, S, D = 1, 2048, 1024
        Df = D // 2 + 1
        return {
            "x": _w((lambda *s: torch.randn(*s, dtype=torch.bfloat16) * 0.2)(B, S, D)),
            "w_r": _w((lambda *s: torch.randn(*s, dtype=torch.bfloat16) * 1.6)(S, Df) * 0.02),
            "w_i": _w((lambda *s: torch.randn(*s, dtype=torch.bfloat16) * 1.6)(S, Df) * 0.02),
        }
    _A_in = _A_setup()

    def _B_setup():
        B, S, Dm, Df = 1, 2048, 1024, 3584
        return {
            "x": _w((lambda *s: torch.randn(*s, dtype=torch.bfloat16) * 0.2)(B, S, Dm)),
            "w_gate": _w((lambda *s: torch.randn(*s, dtype=torch.bfloat16) * 0.8)(Dm, Df) * (Dm ** -0.5)),
            "w_up": _w((lambda *s: torch.randn(*s, dtype=torch.bfloat16) * 0.8)(Dm, Df) * (Dm ** -0.5)),
            "w_down": _w((lambda *s: torch.randn(*s, dtype=torch.bfloat16) * 0.8)(Df, Dm) * (Df ** -0.5)),
        }
    _B_in = _B_setup()

    return {
        "A_x": _A_in["x"],
        "A_w_r": _A_in["w_r"],
        "A_w_i": _A_in["w_i"],
        "B_x": _B_in["x"],
        "B_w_gate": _B_in["w_gate"],
        "B_w_up": _B_in["w_up"],
        "B_w_down": _B_in["w_down"],
    }

def reference(A_x, A_w_r, A_w_i, B_x, B_w_gate, B_w_up, B_w_down):
    def _A_ref(x, w_r, w_i):
        B, S, D = x.shape
        X = (lambda x, axes=(-2,-1): torch.fft.rfft2(x.float(), dim=axes))(x, axes=(-2, -1))
        W = w_r + 1j * w_i
        X = X * W
        y = (lambda x, s, axes=(-2,-1): torch.fft.irfft2(x, s=s, dim=axes).to(torch.bfloat16))(X, (S, D), axes=(-2, -1))
        return y

    def _B_ref(x, w_gate, w_up, w_down):
        g = torch.einsum("bsd,df->bsf", x, w_gate)
        u = torch.einsum("bsd,df->bsf", x, w_up)
        h = _gelu(g) * u
        return torch.einsum("bsf,fd->bsd", h, w_down)

    x = A_x
    xn = _layernorm(x, axis=-1)
    s = _A_ref(xn, A_w_r, A_w_i)
    g = _silu(_B_ref(xn, B_w_gate, B_w_up, B_w_down))
    return x + s * g
```

```cpp
#include <hip/hip_runtime.h>
#include <hip/hip_bf16.h>
#include <stdint.h>

typedef __hip_bfloat16 bf16;
typedef short bf16x8 __attribute__((ext_vector_type(8)));
typedef short bf16x4 __attribute__((ext_vector_type(4)));
typedef float f32x4 __attribute__((ext_vector_type(4)));

#define DEV __device__ __forceinline__

constexpr int SEQ = 2048;
constexpr int DM = 1024;
constexpr int KF = 513;
constexpr int KP = 520;
constexpr int DFF = 3584;

struct cf { float x, y; };

DEV cf cadd(cf a, cf b) { return {a.x + b.x, a.y + b.y}; }
DEV cf csub(cf a, cf b) { return {a.x - b.x, a.y - b.y}; }
DEV cf cmul(cf a, cf b) { return {a.x * b.x - a.y * b.y, a.x * b.y + a.y * b.x}; }
DEV cf cmulc(cf a, cf b) { return {a.x * b.x + a.y * b.y, a.y * b.x - a.x * b.y}; }
DEV cf cconj(cf a) { return {a.x, -a.y}; }

DEV float bfr(float x) {
    unsigned u = __float_as_uint(x);
    u += 0x7fffu + ((u >> 16) & 1u);
    return __uint_as_float(u & 0xffff0000u);
}
DEV unsigned short bf_bits(float x) {
    unsigned u = __float_as_uint(x);
    u += 0x7fffu + ((u >> 16) & 1u);
    return (unsigned short)(u >> 16);
}
DEV float bf_lo(unsigned v) { return __uint_as_float(v << 16); }
DEV float bf_hi(unsigned v) { return __uint_as_float(v & 0xffff0000u); }
DEV float bf2f(bf16 v) { return __uint_as_float(((unsigned)*reinterpret_cast<const unsigned short*>(&v)) << 16); }

DEV constexpr float c16(int j) {
    return j == 0 ? 1.0f : j == 1 ? 0.92387953251128674f : j == 2 ? 0.70710678118654752f : j == 3 ? 0.38268343236508977f
         : j == 4 ? 0.0f : j == 5 ? -0.38268343236508977f : j == 6 ? -0.70710678118654752f : -0.92387953251128674f;
}
DEV constexpr float s16(int j) {
    return j == 0 ? 0.0f : j == 1 ? 0.38268343236508977f : j == 2 ? 0.70710678118654752f : j == 3 ? 0.92387953251128674f
         : j == 4 ? 1.0f : j == 5 ? 0.92387953251128674f : j == 6 ? 0.70710678118654752f : 0.38268343236508977f;
}

template <int N, int SG>
struct Dft {
    static DEV void run(cf* v) {
        cf a[N / 2], b[N / 2];
#pragma unroll
        for (int j = 0; j < N / 2; ++j) {
            a[j] = cadd(v[j], v[j + N / 2]);
            cf d = csub(v[j], v[j + N / 2]);
            const int t = j * (16 / N);
            if (t == 0) {
                b[j] = d;
            } else if (t == 4) {
                b[j] = SG < 0 ? cf{d.y, -d.x} : cf{-d.y, d.x};
            } else {
                const float cr = c16(t), si = SG * s16(t);
                b[j] = {d.x * cr - d.y * si, d.x * si + d.y * cr};
            }
        }
        Dft<N / 2, SG>::run(a);
        Dft<N / 2, SG>::run(b);
#pragma unroll
        for (int r = 0; r < N / 2; ++r) {
            v[2 * r] = a[r];
            v[2 * r + 1] = b[r];
        }
    }
};
template <int SG>
struct Dft<1, SG> {
    static DEV void run(cf*) {}
};

DEV void build_tw2048(cf* tw, int tid, int nthreads) {
    for (int j = tid; j < 2048; j += nthreads) {
        float s, c;
        sincospif((float)j * (1.0f / 1024.0f), &s, &c);
        tw[j] = {c, -s};
    }
}
template <int SG>
DEV cf twv(const cf* tw, int j) {
    cf t = tw[j];
    return SG < 0 ? t : cf{t.x, -t.y};
}

constexpr int RLEN = 576;

template <int SG>
DEV void fft512_wave(cf (&v)[8], cf* ex, const cf* tw, int lane) {
    Dft<8, SG>::run(v);
#pragma unroll
    for (int r = 1; r < 8; ++r) v[r] = cmul(v[r], twv<SG>(tw, 4 * lane * r));
#pragma unroll
    for (int r = 0; r < 8; ++r) ex[r * 68 + lane] = v[r];
    __builtin_amdgcn_wave_barrier();
    const int l0 = lane & 7, rr = lane >> 3;
#pragma unroll
    for (int l1 = 0; l1 < 8; ++l1) v[l1] = ex[rr * 68 + l0 + 8 * l1];
    __builtin_amdgcn_wave_barrier();
    Dft<8, SG>::run(v);
#pragma unroll
    for (int q1 = 1; q1 < 8; ++q1) v[q1] = cmul(v[q1], twv<SG>(tw, 32 * l0 * q1));
#pragma unroll
    for (int q1 = 0; q1 < 8; ++q1) ex[q1 * 66 + lane] = v[q1];
    __builtin_amdgcn_wave_barrier();
    const int r2 = lane & 7, q1s = lane >> 3;
#pragma unroll
    for (int l0i = 0; l0i < 8; ++l0i) v[l0i] = ex[q1s * 66 + l0i + 8 * r2];
    __builtin_amdgcn_wave_barrier();
    Dft<8, SG>::run(v);
}

__global__ __launch_bounds__(512) void k_ln_fft_fwd(const bf16* __restrict__ x, bf16* __restrict__ xn,
                                                    cf* __restrict__ Z1) {
    __shared__ cf tw[2048];
    __shared__ cf R[8][RLEN];
    const int tid = threadIdx.x, lane = tid & 63, b = tid >> 6;
    const int a = blockIdx.x;
    const int row = a + 256 * b;

    const unsigned* xr = reinterpret_cast<const unsigned*>(x + (size_t)row * DM);
    unsigned raw[8];
#pragma unroll
    for (int j = 0; j < 8; ++j) raw[j] = xr[lane + 64 * j];

    build_tw2048(tw, tid, 512);

    float sum = 0.f;
#pragma unroll
    for (int j = 0; j < 8; ++j) sum += bf_lo(raw[j]) + bf_hi(raw[j]);
#pragma unroll
    for (int o = 32; o > 0; o >>= 1) sum += __shfl_xor(sum, o);
    const float mean = sum * (1.0f / DM);
    float sq = 0.f;
#pragma unroll
    for (int j = 0; j < 8; ++j) {
        float d0 = bf_lo(raw[j]) - mean, d1 = bf_hi(raw[j]) - mean;
        sq += d0 * d0 + d1 * d1;
    }
#pragma unroll
    for (int o = 32; o > 0; o >>= 1) sq += __shfl_xor(sq, o);
    const float mb = bfr(mean);
    const float vb = bfr(sq * (1.0f / DM));
    const float den = bfr(sqrtf(bfr(vb + 1e-5f)));

    cf v[8];
    unsigned* xo = reinterpret_cast<unsigned*>(xn + (size_t)row * DM);
#pragma unroll
    for (int j = 0; j < 8; ++j) {
        float e0 = bfr(bfr(bf_lo(raw[j]) - mb) / den);
        float e1 = bfr(bfr(bf_hi(raw[j]) - mb) / den);
        v[j] = {e0, e1};
        xo[lane + 64 * j] = (__float_as_uint(e0) >> 16) | (__float_as_uint(e1) & 0xffff0000u);
    }
    __syncthreads();

    fft512_wave<-1>(v, R[b], tw, lane);
#pragma unroll
    for (int j = 0; j < 8; ++j) R[b][lane + 64 * j] = v[j];
    __syncthreads();

    for (int k = tid; k < KF; k += 512) {
        const int k1 = k & 511, k2 = (512 - k) & 511;
        const cf w = tw[2 * k];
        cf u[8];
#pragma unroll
        for (int bb = 0; bb < 8; ++bb) {
            cf p = R[bb][k1], q = cconj(R[bb][k2]);
            cf e = {0.5f * (p.x + q.x), 0.5f * (p.y + q.y)};
            cf o = {0.5f * (p.x - q.x), 0.5f * (p.y - q.y)};
            cf wo = cmul(w, o);
            u[bb] = {e.x + wo.y, e.y - wo.x};
        }
        Dft<8, -1>::run(u);
#pragma unroll
        for (int bp = 0; bp < 8; ++bp) {
            cf o = bp ? cmul(u[bp], tw[a * bp]) : u[bp];
            Z1[(size_t)(a * 8 + bp) * KP + k] = o;
        }
    }
}

constexpr int CT_LD = 17;
DEV void fft_col_tile(const cf* __restrict__ Z1, cf* __restrict__ Z2, const bf16* __restrict__ wr,
                      const bf16* __restrict__ wi, cf* T, const cf* tw, int bp, int kt, int t) {
    const int k0 = kt * 16;
#pragma unroll
    for (int i = 0; i < 8; ++i) {
        const int a = (t >> 3) + 32 * i, c = t & 7;
        float4 g = float4{0.f, 0.f, 0.f, 0.f};
        if (k0 + 2 * c < KP) g = *reinterpret_cast<const float4*>(Z1 + (size_t)(a * 8 + bp) * KP + k0 + 2 * c);
        T[a * CT_LD + 2 * c] = {g.x, g.y};
        T[a * CT_LD + 2 * c + 1] = {g.z, g.w};
    }
    __syncthreads();
    const int kk = t & 15, j = t >> 4;
    cf v[16];
#pragma unroll
    for (int i = 0; i < 16; ++i) v[i] = T[(j + 16 * i) * CT_LD + kk];
    Dft<16, -1>::run(v);
#pragma unroll
    for (int d = 1; d < 16; ++d) v[d] = cmul(v[d], tw[j * d]);
#pragma unroll
    for (int d = 0; d < 16; ++d) T[(j + 16 * d) * CT_LD + kk] = v[d];
    __syncthreads();
    const int dd = j;
#pragma unroll
    for (int i = 0; i < 16; ++i) v[i] = T[(i + 16 * dd) * CT_LD + kk];
    Dft<16, -1>::run(v);
    const int k = k0 + kk;
    if (k < KF) {
#pragma unroll
        for (int c = 0; c < 16; ++c) {
            const int sp = 8 * (dd + 16 * c) + bp;
            const float w0 = bf2f(wr[(size_t)sp * KF + k]), w1 = bf2f(wi[(size_t)sp * KF + k]);
            v[c] = cmul(v[c], cf{w0, w1});
        }
    }
    Dft<16, 1>::run(v);
#pragma unroll
    for (int i = 1; i < 16; ++i) v[i] = cmulc(v[i], tw[i * dd]);
#pragma unroll
    for (int i = 0; i < 16; ++i) T[(i + 16 * dd) * CT_LD + kk] = v[i];
    __syncthreads();
#pragma unroll
    for (int d = 0; d < 16; ++d) v[d] = T[(j + 16 * d) * CT_LD + kk];
    Dft<16, 1>::run(v);
#pragma unroll
    for (int i = 0; i < 16; ++i) T[(j + 16 * i) * CT_LD + kk] = v[i];
    __syncthreads();
#pragma unroll
    for (int i = 0; i < 8; ++i) {
        const int a = (t >> 3) + 32 * i, c = t & 7;
        const cf p = T[a * CT_LD + 2 * c], q = T[a * CT_LD + 2 * c + 1];
        if (k0 + 2 * c < KP)
            *reinterpret_cast<float4*>(Z2 + (size_t)(a * 8 + bp) * KP + k0 + 2 * c) = float4{p.x, p.y, q.x, q.y};
    }
    __syncthreads();
}

__global__ __launch_bounds__(256) void k_fft_col(const cf* __restrict__ Z1, cf* __restrict__ Z2,
                                                 const bf16* __restrict__ wr, const bf16* __restrict__ wi) {
    __shared__ cf T[256 * CT_LD];
    __shared__ cf tw[256];
    const int t = threadIdx.x;
    {
        float s, c;
        sincospif((float)t * (1.0f / 128.0f), &s, &c);
        tw[t] = {c, -s};
    }
    const int bp = blockIdx.x & 7, kt = blockIdx.x >> 3;
    fft_col_tile(Z1, Z2, wr, wi, T, tw, bp, kt, t);
}

__global__ __launch_bounds__(512) void k_fft_inv_out(const cf* __restrict__ Z2, const bf16* __restrict__ x,
                                                     const float* __restrict__ p0, const float* __restrict__ p1,
                                                     bf16* __restrict__ out) {
    __shared__ cf tw[2048];
    __shared__ cf R[8][RLEN];
    const int tid = threadIdx.x, lane = tid & 63, b = tid >> 6;
    const int a = blockIdx.x;
    build_tw2048(tw, tid, 512);
    __syncthreads();
    for (int k = tid; k < KF; k += 512) {
        cf u[8];
#pragma unroll
        for (int bp = 0; bp < 8; ++bp) {
            cf z = Z2[(size_t)(a * 8 + bp) * KP + k];
            u[bp] = bp ? cmulc(z, tw[a * bp]) : z;
        }
        Dft<8, 1>::run(u);
#pragma unroll
        for (int bb = 0; bb < 8; ++bb) R[bb][k] = u[bb];
    }
    __syncthreads();

    cf v[8];
#pragma unroll
    for (int j = 0; j < 8; ++j) {
        const int k = lane + 64 * j;
        cf p = R[b][k], q = R[b][512 - k];
        if (k == 0) { p.y = 0.f; q.y = 0.f; }
        q = cconj(q);
        cf e = cadd(p, q);
        cf o = cmulc(csub(p, q), tw[2 * k]);
        v[j] = {e.x - o.y, e.y + o.x};
    }
    __builtin_amdgcn_wave_barrier();
    fft512_wave<1>(v, R[b], tw, lane);

    const int row = a + 256 * b;
    const unsigned* xr = reinterpret_cast<const unsigned*>(x + (size_t)row * DM);
    const float2* q0 = reinterpret_cast<const float2*>(p0 + (size_t)row * DM);
    const float2* q1 = reinterpret_cast<const float2*>(p1 + (size_t)row * DM);
    unsigned* orow = reinterpret_cast<unsigned*>(out + (size_t)row * DM);
    const float sc = 1.0f / (2048.0f * 1024.0f);
#pragma unroll
    for (int j = 0; j < 8; ++j) {
        const int n = lane + 64 * j;
        const unsigned xv = xr[n];
        const float2 a0 = q0[n], a1 = q1[n];
        const float d0 = bfr(a0.x + a1.x), d1 = bfr(a0.y + a1.y);
        const float g0 = bfr(d0 / (1.0f + __expf(-d0))), g1 = bfr(d1 / (1.0f + __expf(-d1)));
        const float s0 = bfr(v[j].x * sc), s1 = bfr(v[j].y * sc);
        const float o0 = bfr(bf_lo(xv) + bfr(s0 * g0)), o1 = bfr(bf_hi(xv) + bfr(s1 * g1));
        orow[n] = (__float_as_uint(o0) >> 16) | (__float_as_uint(o1) & 0xffff0000u);
    }
}

DEV int lds_byte(int r, int c) {
    int st = (r >> 4) * 2 + (c >> 5), rr = r & 15, cc = c & 31, ob = rr * 64 + cc * 2;
    return st * 1024 + (ob ^ (((ob >> 9) & 1) << 5));
}
DEV void stage_rc(int b, int& R, int& C) {
    int st = b / 1024, sb = b % 1024, swz = sb ^ (((sb >> 9) & 1) << 5);
    R = (st >> 1) * 16 + swz / 64;
    C = (st & 1) * 32 + (swz % 64) / 2;
}
DEV int b_off(int row, int ch) { return 256 * row + 16 * (ch ^ (((row & 3) << 2) | ((row >> 2) & 3))); }

typedef __attribute__((address_space(3))) bf16x4* lds_b4_ptr;
DEV bf16x8 ld_frag_tr(const char* base, int c, int ks, int lane) {
    const int g = lane >> 4, q = (lane & 15) >> 2, p = lane & 3;
    const int o0 = b_off(32 * ks + 8 * g + q, 2 * c + (p >> 1)) + 8 * (p & 1);
    const int o1 = b_off(32 * ks + 8 * g + 4 + q, 2 * c + (p >> 1)) + 8 * (p & 1);
    bf16x4 lo = __builtin_amdgcn_ds_read_tr16_b64_v4i16((lds_b4_ptr)(base + o0));
    bf16x4 hi = __builtin_amdgcn_ds_read_tr16_b64_v4i16((lds_b4_ptr)(base + o1));
    bf16x8 r;
    r[0] = lo[0]; r[1] = lo[1]; r[2] = lo[2]; r[3] = lo[3];
    r[4] = hi[0]; r[5] = hi[1]; r[6] = hi[2]; r[7] = hi[3];
    return r;
}

DEV float gelu_tanh(float x) {
    const float u = 0.7978845608028654f * (x + 0.044715f * x * x * x);
    return 0.5f * x * (1.0f + tanhf(u));
}

#define WAIT_V(n) asm volatile("s_waitcnt vmcnt(" #n ")" ::: "memory")
#define WAIT_L(n) asm volatile("s_waitcnt lgkmcnt(" #n ")" ::: "memory")
#define BAR __builtin_amdgcn_s_barrier()
#define SCHED __builtin_amdgcn_sched_barrier(0)

constexpr int G1_HT = 128 * 64;
constexpr int G1_SHM = 8 * G1_HT * 2;

__global__ __launch_bounds__(512, 2) void k_gemm1(const bf16* __restrict__ A, const bf16* __restrict__ Wg,
                                                  const bf16* __restrict__ Wu, bf16* __restrict__ H) {
    extern __shared__ __attribute__((aligned(16))) bf16 shm[];
    constexpr int K = DM, LDB = DFF;
#define SA(b, h) (shm + ((b) * 2 + (h)) * G1_HT)
#define SB(b, h) (shm + (4 + (b) * 2 + (h)) * G1_HT)
#define STAGE_A_X(T, P, br, kt)                                                                   \
    do {                                                                                          \
        const bf16* _g = A + (size_t)(br) * K + (kt) * 64;                                        \
        for (int _i = 0; _i < 2; ++_i) {                                                          \
            int _b = (T) * 16 + _i * 8192, _r, _c;                                                \
            stage_rc(_b, _r, _c);                                                                 \
            __builtin_amdgcn_global_load_lds((const unsigned*)(_g + (size_t)_r * K + _c),         \
                                             (unsigned*)((char*)(P) + _b), 16, 0, 0);             \
        }                                                                                         \
    } while (0)
#define STAGE_A(P, br, kt) STAGE_A_X(threadIdx.x, P, br, kt)
#define STAGE_B(P, W, kt)                                                                         \
    do {                                                                                          \
        const bf16* _g = (W) + (size_t)(kt) * 64 * LDB + ncol0;                                   \
        for (int _i = 0; _i < 2; ++_i) {                                                          \
            int _b = threadIdx.x * 16 + _i * 8192, _r = _b >> 8, _p = (_b >> 4) & 15;             \
            int _ch = _p ^ (((_r & 3) << 2) | ((_r >> 2) & 3));                                   \
            __builtin_amdgcn_global_load_lds((const unsigned*)(_g + (size_t)_r * LDB + _ch * 8),  \
                                             (unsigned*)((char*)(P) + _b), 16, 0, 0);             \
        }                                                                                         \
    } while (0)
#define LDA(dst, b, h)                                                                            \
    for (int m = 0; m < 4; ++m)                                                                   \
        for (int k = 0; k < 2; ++k)                                                               \
    dst[m][k] = *reinterpret_cast<const bf16x8*>((char*)SA(b, h) + lds_byte(wr * 64 + m * 16 + fr, k * 32 + fq * 8))
#define LDB_(dst, b, h)                                                                           \
    for (int n = 0; n < 2; ++n)                                                                   \
        for (int k = 0; k < 2; ++k) dst[n][k] = ld_frag_tr((const char*)SB(b, h), wc * 2 + n, k, lane)
#define MMA(ai, bj, At, Bt)                                                                       \
    do {                                                                                          \
        __builtin_amdgcn_s_setprio(1);                                                            \
        for (int m = 0; m < 4; ++m)                                                               \
            for (int n = 0; n < 2; ++n)                                                           \
                for (int k = 0; k < 2; ++k)                                                       \
                    acc[ai][bj][m][n] =                                                           \
                        __builtin_amdgcn_mfma_f32_16x16x32_bf16(Bt[n][k], At[m][k], acc[ai][bj][m][n], 0, 0, 0); \
        __builtin_amdgcn_s_setprio(0);                                                            \
    } while (0)

    const int nM = SEQ / 256;
    const int wgid = blockIdx.x;
    const int pm = wgid % nM, pn = wgid / nM;
    const int brow = pm * 256, ncol0 = pn * 128;
    int wid = threadIdx.x >> 6, lane = threadIdx.x & 63, wr = wid >> 2, wc = wid & 3, fr = lane & 15, fq = lane >> 4;
    f32x4 acc[2][2][4][2] = {};
    bf16x8 At[4][2], B0[2][2], B1[2][2];
    constexpr int nt = K / 64;

    STAGE_B(SB(0, 0), Wg, 0); STAGE_A(SA(0, 0), brow, 0);
    STAGE_B(SB(0, 1), Wu, 0); STAGE_A(SA(0, 1), brow + 128, 0);
    if (wr == 1) BAR;
    WAIT_V(4); BAR;
    STAGE_B(SB(1, 0), Wg, 1); STAGE_A(SA(1, 0), brow, 1); STAGE_B(SB(1, 1), Wu, 1);
    WAIT_V(6); BAR;
    for (int t = 0; t < nt - 2; t += 2) {
        LDB_(B0, 0, 0); SCHED; LDA(At, 0, 0); STAGE_A(SA(1, 1), brow + 128, t + 1);
        WAIT_L(8); BAR; WAIT_L(0); MMA(0, 0, At, B0); BAR; SCHED;
        LDB_(B1, 0, 1); STAGE_B(SB(0, 0), Wg, t + 2);
        BAR; WAIT_L(0); MMA(0, 1, At, B1); BAR;
        LDA(At, 0, 1); STAGE_A(SA(0, 0), brow, t + 2);
        BAR; WAIT_L(0); MMA(1, 0, At, B0); BAR; SCHED;
        STAGE_B(SB(0, 1), Wu, t + 2);
        WAIT_V(6); BAR; MMA(1, 1, At, B1); BAR;
        LDB_(B0, 1, 0); SCHED; LDA(At, 1, 0); STAGE_A(SA(0, 1), brow + 128, t + 2);
        WAIT_L(8); BAR; WAIT_L(0); MMA(0, 0, At, B0); BAR; SCHED;
        LDB_(B1, 1, 1); STAGE_B(SB(1, 0), Wg, t + 3);
        BAR; WAIT_L(0); MMA(0, 1, At, B1); BAR;
        LDA(At, 1, 1); STAGE_A(SA(1, 0), brow, t + 3);
        BAR; WAIT_L(0); MMA(1, 0, At, B0); BAR; SCHED;
        STAGE_B(SB(1, 1), Wu, t + 3);
        WAIT_V(6); BAR; MMA(1, 1, At, B1); BAR;
    }
    int tz = threadIdx.x;
    asm volatile("" : "+v"(tz));
    wid = tz >> 6; lane = tz & 63; wr = wid >> 2; wc = wid & 3; fr = lane & 15; fq = lane >> 4;
    {
        LDB_(B0, 0, 0); WAIT_V(0); LDA(At, 0, 0); STAGE_A_X(tz, SA(1, 1), brow + 128, nt - 1);
        BAR; WAIT_L(0); MMA(0, 0, At, B0); BAR;
        LDB_(B1, 0, 1); BAR; WAIT_L(0); MMA(0, 1, At, B1); BAR;
        LDA(At, 0, 1); WAIT_V(4); BAR; WAIT_L(0); MMA(1, 0, At, B0); MMA(1, 1, At, B1); BAR;
    }
    {
        LDB_(B0, 1, 0); LDA(At, 1, 0); WAIT_V(2); BAR; WAIT_L(0); MMA(0, 0, At, B0); BAR;
        LDB_(B1, 1, 1); WAIT_V(0); BAR; WAIT_L(0); MMA(0, 1, At, B1); BAR;
        LDA(At, 1, 1); BAR; WAIT_L(0); MMA(1, 0, At, B0); MMA(1, 1, At, B1); BAR;
    }
    if (wr == 0) BAR;
#pragma unroll
    for (int ai = 0; ai < 2; ++ai)
#pragma unroll
        for (int m = 0; m < 4; ++m)
#pragma unroll
            for (int n = 0; n < 2; ++n) {
                const int row = brow + ai * 128 + wr * 64 + m * 16 + fr;
                const int col = ncol0 + wc * 32 + n * 16 + fq * 4;
                unsigned short hv[4];
#pragma unroll
                for (int j = 0; j < 4; ++j) {
                    const float g = bfr(acc[ai][0][m][n][j]);
                    const float u = bfr(acc[ai][1][m][n][j]);
                    hv[j] = bf_bits(bfr(gelu_tanh(g)) * u);
                }
                uint2 pk;
                pk.x = (unsigned)hv[0] | ((unsigned)hv[1] << 16);
                pk.y = (unsigned)hv[2] | ((unsigned)hv[3] << 16);
                *reinterpret_cast<uint2*>(H + (size_t)row * DFF + col) = pk;
            }
#undef SA
#undef SB
#undef STAGE_A_X
#undef STAGE_A
#undef STAGE_B
#undef LDA
#undef LDB_
#undef MMA
}

__global__ __launch_bounds__(256) void k_gemm2(const bf16* __restrict__ A, const bf16* __restrict__ W,
                                               float* __restrict__ P0, float* __restrict__ P1) {
    __shared__ __attribute__((aligned(16))) char lds[2][32768];
    constexpr int K = DFF, LDB = DM, KH = K / 2;
    const int wgid = blockIdx.x;
    const int sk = wgid & 1, tile = wgid >> 1;
    const int pm = tile & 15, pn = tile >> 4;
    const int brow = pm * 128, ncol0 = pn * 128, kbase = sk * KH;
    const int tid = threadIdx.x, wid = tid >> 6, lane = tid & 63, wr = wid >> 1, wc = wid & 1, fr = lane & 15,
              fq = lane >> 4;
    f32x4 acc[4][4] = {};
    constexpr int nt = KH / 64;

    auto stage = [&](int buf, int kt) {
        char* pa = lds[buf];
        char* pb = lds[buf] + 16384;
        const bf16* ga = A + (size_t)brow * K + kbase + kt * 64;
        const bf16* gb = W + (size_t)(kbase + kt * 64) * LDB + ncol0;
#pragma unroll
        for (int i = 0; i < 4; ++i) {
            int b = tid * 16 + i * 4096, r, c;
            stage_rc(b, r, c);
            __builtin_amdgcn_global_load_lds((const unsigned*)(ga + (size_t)r * K + c), (unsigned*)(pa + b), 16, 0, 0);
        }
#pragma unroll
        for (int i = 0; i < 4; ++i) {
            int b = tid * 16 + i * 4096, r = b >> 8, p = (b >> 4) & 15;
            int ch = p ^ (((r & 3) << 2) | ((r >> 2) & 3));
            __builtin_amdgcn_global_load_lds((const unsigned*)(gb + (size_t)r * LDB + ch * 8), (unsigned*)(pb + b), 16, 0, 0);
        }
    };

    stage(0, 0);
    WAIT_V(0);
    __syncthreads();
    for (int t = 0; t < nt; ++t) {
        const int cur = t & 1;
        if (t + 1 < nt) stage(cur ^ 1, t + 1);
        bf16x8 af[4][2], bfm[4][2];
#pragma unroll
        for (int m = 0; m < 4; ++m)
#pragma unroll
            for (int k = 0; k < 2; ++k)
                af[m][k] = *reinterpret_cast<const bf16x8*>(lds[cur] + lds_byte(wr * 64 + m * 16 + fr, k * 32 + fq * 8));
#pragma unroll
        for (int n = 0; n < 4; ++n)
#pragma unroll
            for (int k = 0; k < 2; ++k) bfm[n][k] = ld_frag_tr(lds[cur] + 16384, wc * 4 + n, k, lane);
#pragma unroll
        for (int m = 0; m < 4; ++m)
#pragma unroll
            for (int n = 0; n < 4; ++n)
#pragma unroll
                for (int k = 0; k < 2; ++k)
                    acc[m][n] = __builtin_amdgcn_mfma_f32_16x16x32_bf16(bfm[n][k], af[m][k], acc[m][n], 0, 0, 0);
        WAIT_V(0);
        __syncthreads();
    }
    float* P = sk ? P1 : P0;
#pragma unroll
    for (int m = 0; m < 4; ++m)
#pragma unroll
        for (int n = 0; n < 4; ++n) {
            const int row = brow + wr * 64 + m * 16 + fr;
            const int col = ncol0 + wc * 64 + n * 16 + fq * 4;
            *reinterpret_cast<f32x4*>(P + (size_t)row * DM + col) = acc[m][n];
        }
}

extern "C" void kernel_launch(void* const* d_in, const int* in_sizes, int n_in, void* d_out, int out_size,
                              void* d_ws, size_t ws_size, hipStream_t stream) {
    const bf16* x = (const bf16*)d_in[0];
    const bf16* wre = (const bf16*)d_in[1];
    const bf16* wim = (const bf16*)d_in[2];
    const bf16* wg = (const bf16*)d_in[4];
    const bf16* wu = (const bf16*)d_in[5];
    const bf16* wd = (const bf16*)d_in[6];
    bf16* out = (bf16*)d_out;

    char* ws = (char*)d_ws;
    size_t off = 0;
    auto take = [&](size_t bytes) {
        char* p = ws + off;
        off += (bytes + 255) & ~(size_t)255;
        return p;
    };
    bf16* xn = (bf16*)take((size_t)SEQ * DM * 2);
    bf16* h = (bf16*)take((size_t)SEQ * DFF * 2);
    cf* Z1 = (cf*)take((size_t)SEQ * KP * 8);
    cf* Z2 = (cf*)take((size_t)SEQ * KP * 8);
    float* p0 = (float*)take((size_t)SEQ * DM * 4);
    float* p1 = (float*)take((size_t)SEQ * DM * 4);

    (void)hipFuncSetAttribute((const void*)k_gemm1, hipFuncAttributeMaxDynamicSharedMemorySize, G1_SHM);

    k_ln_fft_fwd<<<256, 512, 0, stream>>>(x, xn, Z1);
    k_gemm1<<<224, 512, G1_SHM, stream>>>(xn, wg, wu, h);
    k_fft_col<<<8 * 33, 256, 0, stream>>>(Z1, Z2, wre, wim);
    k_gemm2<<<256, 256, 0, stream>>>(h, wd, p0, p1);
    k_fft_inv_out<<<256, 512, 0, stream>>>(Z2, x, p0, p1, out);
}
```

```cpp
#include <hip/hip_runtime.h>
#include <hip/hip_bf16.h>
#include <stdint.h>

typedef __hip_bfloat16 bf16;
typedef short bf16x8 __attribute__((ext_vector_type(8)));
typedef short bf16x4 __attribute__((ext_vector_type(4)));
typedef float f32x4 __attribute__((ext_vector_type(4)));

#define DEV __device__ __forceinline__

constexpr int SEQ = 2048;
constexpr int DM = 1024;
constexpr int KF = 513;
constexpr int KP = 520;
constexpr int DFF = 3584;

struct cf { float x, y; };

DEV cf cadd(cf a, cf b) { return {a.x + b.x, a.y + b.y}; }
DEV cf csub(cf a, cf b) { return {a.x - b.x, a.y - b.y}; }
DEV cf cmul(cf a, cf b) { return {a.x * b.x - a.y * b.y, a.x * b.y + a.y * b.x}; }
DEV cf cmulc(cf a, cf b) { return {a.x * b.x + a.y * b.y, a.y * b.x - a.x * b.y}; }
DEV cf cconj(cf a) { return {a.x, -a.y}; }

DEV float bfr(float x) {
    unsigned u = __float_as_uint(x);
    u += 0x7fffu + ((u >> 16) & 1u);
    return __uint_as_float(u & 0xffff0000u);
}
DEV unsigned short bf_bits(float x) {
    unsigned u = __float_as_uint(x);
    u += 0x7fffu + ((u >> 16) & 1u);
    return (unsigned short)(u >> 16);
}
DEV float bf_lo(unsigned v) { return __uint_as_float(v << 16); }
DEV float bf_hi(unsigned v) { return __uint_as_float(v & 0xffff0000u); }
DEV float bf2f(bf16 v) { return __uint_as_float(((unsigned)*reinterpret_cast<const unsigned short*>(&v)) << 16); }

DEV constexpr float c16(int j) {
    return j == 0 ? 1.0f : j == 1 ? 0.92387953251128674f : j == 2 ? 0.70710678118654752f : j == 3 ? 0.38268343236508977f
         : j == 4 ? 0.0f : j == 5 ? -0.38268343236508977f : j == 6 ? -0.70710678118654752f : -0.92387953251128674f;
}
DEV constexpr float s16(int j) {
    return j == 0 ? 0.0f : j == 1 ? 0.38268343236508977f : j == 2 ? 0.70710678118654752f : j == 3 ? 0.92387953251128674f
         : j == 4 ? 1.0f : j == 5 ? 0.92387953251128674f : j == 6 ? 0.70710678118654752f : 0.38268343236508977f;
}

template <int N, int SG>
struct Dft {
    static DEV void run(cf* v) {
        cf a[N / 2], b[N / 2];
#pragma unroll
        for (int j = 0; j < N / 2; ++j) {
            a[j] = cadd(v[j], v[j + N / 2]);
            cf d = csub(v[j], v[j + N / 2]);
            const int t = j * (16 / N);
            if (t == 0) {
                b[j] = d;
            } else if (t == 4) {
                b[j] = SG < 0 ? cf{d.y, -d.x} : cf{-d.y, d.x};
            } else {
                const float cr = c16(t), si = SG * s16(t);
                b[j] = {d.x * cr - d.y * si, d.x * si + d.y * cr};
            }
        }
        Dft<N / 2, SG>::run(a);
        Dft<N / 2, SG>::run(b);
#pragma unroll
        for (int r = 0; r < N / 2; ++r) {
            v[2 * r] = a[r];
            v[2 * r + 1] = b[r];
        }
    }
};
template <int SG>
struct Dft<1, SG> {
    static DEV void run(cf*) {}
};

DEV void build_tw2048(cf* tw, int tid, int nthreads) {
    for (int j = tid; j < 2048; j += nthreads) {
        float s, c;
        sincospif((float)j * (1.0f / 1024.0f), &s, &c);
        tw[j] = {c, -s};
    }
}
template <int SG>
DEV cf twv(const cf* tw, int j) {
    cf t = tw[j];
    return SG < 0 ? t : cf{t.x, -t.y};
}

constexpr int RLEN = 576;

template <int SG>
DEV void fft512_wave(cf (&v)[8], cf* ex, const cf* tw, int lane) {
    Dft<8, SG>::run(v);
#pragma unroll
    for (int r = 1; r < 8; ++r) v[r] = cmul(v[r], twv<SG>(tw, 4 * lane * r));
#pragma unroll
    for (int r = 0; r < 8; ++r) ex[r * 68 + lane] = v[r];
    __builtin_amdgcn_wave_barrier();
    const int l0 = lane & 7, rr = lane >> 3;
#pragma unroll
    for (int l1 = 0; l1 < 8; ++l1) v[l1] = ex[rr * 68 + l0 + 8 * l1];
    __builtin_amdgcn_wave_barrier();
    Dft<8, SG>::run(v);
#pragma unroll
    for (int q1 = 1; q1 < 8; ++q1) v[q1] = cmul(v[q1], twv<SG>(tw, 32 * l0 * q1));
#pragma unroll
    for (int q1 = 0; q1 < 8; ++q1) ex[q1 * 66 + lane] = v[q1];
    __builtin_amdgcn_wave_barrier();
    const int r2 = lane & 7, q1s = lane >> 3;
#pragma unroll
    for (int l0i = 0; l0i < 8; ++l0i) v[l0i] = ex[q1s * 66 + l0i + 8 * r2];
    __builtin_amdgcn_wave_barrier();
    Dft<8, SG>::run(v);
}

__global__ __launch_bounds__(512) void k_ln_fft_fwd(const bf16* __restrict__ x, bf16* __restrict__ xn,
                                                    cf* __restrict__ Z1) {
    __shared__ cf tw[2048];
    __shared__ cf R[8][RLEN];
    const int tid = threadIdx.x, lane = tid & 63, b = tid >> 6;
    const int a = blockIdx.x;
    const int row = a + 256 * b;

    const unsigned* xr = reinterpret_cast<const unsigned*>(x + (size_t)row * DM);
    unsigned raw[8];
#pragma unroll
    for (int j = 0; j < 8; ++j) raw[j] = xr[lane + 64 * j];

    build_tw2048(tw, tid, 512);

    float sum = 0.f;
#pragma unroll
    for (int j = 0; j < 8; ++j) sum += bf_lo(raw[j]) + bf_hi(raw[j]);
#pragma unroll
    for (int o = 32; o > 0; o >>= 1) sum += __shfl_xor(sum, o);
    const float mean = sum * (1.0f / DM);
    float sq = 0.f;
#pragma unroll
    for (int j = 0; j < 8; ++j) {
        float d0 = bf_lo(raw[j]) - mean, d1 = bf_hi(raw[j]) - mean;
        sq += d0 * d0 + d1 * d1;
    }
#pragma unroll
    for (int o = 32; o > 0; o >>= 1) sq += __shfl_xor(sq, o);
    const float mb = bfr(mean);
    const float vb = bfr(sq * (1.0f / DM));
    const float den = bfr(sqrtf(bfr(vb + 1e-5f)));

    cf v[8];
    unsigned* xo = reinterpret_cast<unsigned*>(xn + (size_t)row * DM);
#pragma unroll
    for (int j = 0; j < 8; ++j) {
        float e0 = bfr(bfr(bf_lo(raw[j]) - mb) / den);
        float e1 = bfr(bfr(bf_hi(raw[j]) - mb) / den);
        v[j] = {e0, e1};
        xo[lane + 64 * j] = (__float_as_uint(e0) >> 16) | (__float_as_uint(e1) & 0xffff0000u);
    }
    __syncthreads();

    fft512_wave<-1>(v, R[b], tw, lane);
#pragma unroll
    for (int j = 0; j < 8; ++j) R[b][lane + 64 * j] = v[j];
    __syncthreads();

    for (int k = tid; k < KF; k += 512) {
        const int k1 = k & 511, k2 = (512 - k) & 511;
        const cf w = tw[2 * k];
        cf u[8];
#pragma unroll
        for (int bb = 0; bb < 8; ++bb) {
            cf p = R[bb][k1], q = cconj(R[bb][k2]);
            cf e = {0.5f * (p.x + q.x), 0.5f * (p.y + q.y)};
            cf o = {0.5f * (p.x - q.x), 0.5f * (p.y - q.y)};
            cf wo = cmul(w, o);
            u[bb] = {e.x + wo.y, e.y - wo.x};
        }
        Dft<8, -1>::run(u);
#pragma unroll
        for (int bp = 0; bp < 8; ++bp) {
            cf o = bp ? cmul(u[bp], tw[a * bp]) : u[bp];
            Z1[(size_t)(a * 8 + bp) * KP + k] = o;
        }
    }
}

constexpr int CT_LD = 17;
DEV void fft_col_tile(const cf* __restrict__ Z1, cf* __restrict__ Z2, const bf16* __restrict__ wr,
                      const bf16* __restrict__ wi, cf* T, const cf* tw, int bp, int kt, int t) {
    const int k0 = kt * 16;
    const int kk = t & 15, j = t >> 4;
    const int kcl = (k0 + kk < KF) ? (k0 + kk) : 0;
    unsigned short wq0[16], wq1[16];
#pragma unroll
    for (int c = 0; c < 16; ++c) {
        const int sp = 8 * (j + 16 * c) + bp;
        wq0[c] = *reinterpret_cast<const unsigned short*>(wr + (size_t)sp * KF + kcl);
        wq1[c] = *reinterpret_cast<const unsigned short*>(wi + (size_t)sp * KF + kcl);
    }
#pragma unroll
    for (int i = 0; i < 8; ++i) {
        const int a = (t >> 3) + 32 * i, c = t & 7;
        float4 g = float4{0.f, 0.f, 0.f, 0.f};
        if (k0 + 2 * c < KP) g = *reinterpret_cast<const float4*>(Z1 + (size_t)(a * 8 + bp) * KP + k0 + 2 * c);
        T[a * CT_LD + 2 * c] = {g.x, g.y};
        T[a * CT_LD + 2 * c + 1] = {g.z, g.w};
    }
    __syncthreads();
    cf v[16];
#pragma unroll
    for (int i = 0; i < 16; ++i) v[i] = T[(j + 16 * i) * CT_LD + kk];
    Dft<16, -1>::run(v);
#pragma unroll
    for (int d = 1; d < 16; ++d) v[d] = cmul(v[d], tw[j * d]);
#pragma unroll
    for (int d = 0; d < 16; ++d) T[(j + 16 * d) * CT_LD + kk] = v[d];
    __syncthreads();
    const int dd = j;
#pragma unroll
    for (int i = 0; i < 16; ++i) v[i] = T[(i + 16 * dd) * CT_LD + kk];
    Dft<16, -1>::run(v);
#pragma unroll
    for (int c = 0; c < 16; ++c)
        v[c] = cmul(v[c], cf{__uint_as_float((unsigned)wq0[c] << 16), __uint_as_float((unsigned)wq1[c] << 16)});
    Dft<16, 1>::run(v);
#pragma unroll
    for (int i = 1; i < 16; ++i) v[i] = cmulc(v[i], tw[i * dd]);
#pragma unroll
    for (int i = 0; i < 16; ++i) T[(i + 16 * dd) * CT_LD + kk] = v[i];
    __syncthreads();
#pragma unroll
    for (int d = 0; d < 16; ++d) v[d] = T[(j + 16 * d) * CT_LD + kk];
    Dft<16, 1>::run(v);
#pragma unroll
    for (int i = 0; i < 16; ++i) T[(j + 16 * i) * CT_LD + kk] = v[i];
    __syncthreads();
#pragma unroll
    for (int i = 0; i < 8; ++i) {
        const int a = (t >> 3) + 32 * i, c = t & 7;
        const cf p = T[a * CT_LD + 2 * c], q = T[a * CT_LD + 2 * c + 1];
        if (k0 + 2 * c < KP)
            *reinterpret_cast<float4*>(Z2 + (size_t)(a * 8 + bp) * KP + k0 + 2 * c) = float4{p.x, p.y, q.x, q.y};
    }
    __syncthreads();
}

__global__ __launch_bounds__(256) void k_fft_col(const cf* __restrict__ Z1, cf* __restrict__ Z2,
                                                 const bf16* __restrict__ wr, const bf16* __restrict__ wi) {
    __shared__ cf T[256 * CT_LD];
    __shared__ cf tw[256];
    const int t = threadIdx.x;
    {
        float s, c;
        sincospif((float)t * (1.0f / 128.0f), &s, &c);
        tw[t] = {c, -s};
    }
    const int bp = blockIdx.x & 7, kt = blockIdx.x >> 3;
    fft_col_tile(Z1, Z2, wr, wi, T, tw, bp, kt, t);
}

__global__ __launch_bounds__(512) void k_fft_inv_out(const cf* __restrict__ Z2, const bf16* __restrict__ x,
                                                     const float* __restrict__ p0, const float* __restrict__ p1,
                                                     bf16* __restrict__ out) {
    __shared__ cf tw[2048];
    __shared__ cf R[8][RLEN];
    const int tid = threadIdx.x, lane = tid & 63, b = tid >> 6;
    const int a = blockIdx.x;
    build_tw2048(tw, tid, 512);
    __syncthreads();
    for (int k = tid; k < KF; k += 512) {
        cf u[8];
#pragma unroll
        for (int bp = 0; bp < 8; ++bp) {
            cf z = Z2[(size_t)(a * 8 + bp) * KP + k];
            u[bp] = bp ? cmulc(z, tw[a * bp]) : z;
        }
        Dft<8, 1>::run(u);
#pragma unroll
        for (int bb = 0; bb < 8; ++bb) R[bb][k] = u[bb];
    }
    __syncthreads();

    cf v[8];
#pragma unroll
    for (int j = 0; j < 8; ++j) {
        const int k = lane + 64 * j;
        cf p = R[b][k], q = R[b][512 - k];
        if (k == 0) { p.y = 0.f; q.y = 0.f; }
        q = cconj(q);
        cf e = cadd(p, q);
        cf o = cmulc(csub(p, q), tw[2 * k]);
        v[j] = {e.x - o.y, e.y + o.x};
    }
    __builtin_amdgcn_wave_barrier();
    fft512_wave<1>(v, R[b], tw, lane);

    const int row = a + 256 * b;
    const unsigned* xr = reinterpret_cast<const unsigned*>(x + (size_t)row * DM);
    const float2* q0 = reinterpret_cast<const float2*>(p0 + (size_t)row * DM);
    const float2* q1 = reinterpret_cast<const float2*>(p1 + (size_t)row * DM);
    unsigned* orow = reinterpret_cast<unsigned*>(out + (size_t)row * DM);
    const float sc = 1.0f / (2048.0f * 1024.0f);
#pragma unroll
    for (int j = 0; j < 8; ++j) {
        const int n = lane + 64 * j;
        const unsigned xv = xr[n];
        const float2 a0 = q0[n], a1 = q1[n];
        const float d0 = bfr(a0.x + a1.x), d1 = bfr(a0.y + a1.y);
        const float g0 = bfr(d0 / (1.0f + __expf(-d0))), g1 = bfr(d1 / (1.0f + __expf(-d1)));
        const float s0 = bfr(v[j].x * sc), s1 = bfr(v[j].y * sc);
        const float o0 = bfr(bf_lo(xv) + bfr(s0 * g0)), o1 = bfr(bf_hi(xv) + bfr(s1 * g1));
        orow[n] = (__float_as_uint(o0) >> 16) | (__float_as_uint(o1) & 0xffff0000u);
    }
}

DEV int lds_byte(int r, int c) {
    int st = (r >> 4) * 2 + (c >> 5), rr = r & 15, cc = c & 31, ob = rr * 64 + cc * 2;
    return st * 1024 + (ob ^ (((ob >> 9) & 1) << 5));
}
DEV void stage_rc(int b, int& R, int& C) {
    int st = b / 1024, sb = b % 1024, swz = sb ^ (((sb >> 9) & 1) << 5);
    R = (st >> 1) * 16 + swz / 64;
    C = (st & 1) * 32 + (swz % 64) / 2;
}
DEV int b_off(int row, int ch) { return 256 * row + 16 * (ch ^ (((row & 3) << 2) | ((row >> 2) & 3))); }

DEV unsigned tr_off(int c, int ks, int t, int lane) {
    const int g = lane >> 4, q = (lane & 15) >> 2, p = lane & 3;
    return (unsigned)(b_off(32 * ks + 8 * g + 4 * t + q, 2 * c + (p >> 1)) + 8 * (p & 1));
}
#define TR_READ(dst, addr, OFF) \
    asm volatile("ds_read_b64_tr_b16 %0, %1 offset:%2" : "=v"(dst) : "v"(addr), "i"(OFF))
#define PIN(x) asm volatile("" : "+v"(x))
DEV bf16x8 cat8(bf16x4 lo, bf16x4 hi) {
    bf16x8 r;
    r[0] = lo[0]; r[1] = lo[1]; r[2] = lo[2]; r[3] = lo[3];
    r[4] = hi[0]; r[5] = hi[1]; r[6] = hi[2]; r[7] = hi[3];
    return r;
}

DEV float gelu_tanh(float x) {
    const float u2 = 1.5957691216057308f * (x + 0.044715f * x * x * x);
    return x * __frcp_rn(1.0f + __expf(-u2));
}
DEV unsigned pack_bf16(float a, float b);
DEV unsigned geglu_pair(float g0, float g1, float u0, float u1) {
    const unsigned gp = pack_bf16(g0, g1), up = pack_bf16(u0, u1);
    const float a0 = bf_lo(gp), a1 = bf_hi(gp);
    const float c1 = -1.5957691216057308f * 1.4426950408889634f, c3 = c1 * 0.044715f;
    const float e0 = __builtin_amdgcn_exp2f(a0 * fmaf(a0 * a0, c3, c1));
    const float e1 = __builtin_amdgcn_exp2f(a1 * fmaf(a1 * a1, c3, c1));
    const float t0 = a0 * __builtin_amdgcn_rcpf(1.0f + e0), t1 = a1 * __builtin_amdgcn_rcpf(1.0f + e1);
    const unsigned tp = pack_bf16(t0, t1);
    return pack_bf16(bf_lo(tp) * bf_lo(up), bf_hi(tp) * bf_hi(up));
}
DEV unsigned pack_bf16(float a, float b) {
    unsigned r;
    asm("v_cvt_pk_bf16_f32 %0, %1, %2" : "=v"(r) : "v"(a), "v"(b));
    return r;
}

#define WAIT_V(n) asm volatile("s_waitcnt vmcnt(" #n ")" ::: "memory")
#define WAIT_L(n) asm volatile("s_waitcnt lgkmcnt(" #n ")" ::: "memory")
#define BAR __builtin_amdgcn_s_barrier()
#define SCHED __builtin_amdgcn_sched_barrier(0)

constexpr int G1_HT = 128 * 64;
constexpr int G1_SHM = 8 * G1_HT * 2;

__global__ __launch_bounds__(512, 2) void k_gemm1(const bf16* __restrict__ A, const bf16* __restrict__ Wg,
                                                  const bf16* __restrict__ Wu, bf16* __restrict__ H,
                                                  const cf* __restrict__ Z1, cf* __restrict__ Z2,
                                                  const bf16* __restrict__ wre, const bf16* __restrict__ wim) {
    extern __shared__ __attribute__((aligned(16))) bf16 shm[];
    if (blockIdx.x >= 224) {
        const uint4* src = reinterpret_cast<const uint4*>(wre);
        const int n16 = DFF * DM * 2 / 16;
        unsigned accx = 0;
        for (int i = (blockIdx.x - 224) * 512 + threadIdx.x; i < n16; i += 32 * 512) {
            const uint4 v = src[i];
            accx ^= v.x ^ v.y ^ v.z ^ v.w;
        }
        if (accx == 0x9e3779b9u && threadIdx.x == 0x7fffffff) *reinterpret_cast<unsigned*>(Z2) = accx;
        return;
    }
    constexpr int K = DM, LDB = DFF;
#define SA(b, h) (shm + (4 + (b) * 2 + (h)) * G1_HT)
#define SB(b, h) (shm + ((b) * 2 + (h)) * G1_HT)
#define STAGE_A_X(T, P, br, kt)                                                                   \
    do {                                                                                          \
        const bf16* _g = A + (size_t)(br) * K + (kt) * 64;                                        \
        for (int _i = 0; _i < 2; ++_i) {                                                          \
            int _b = (T) * 16 + _i * 8192, _r, _c;                                                \
            stage_rc(_b, _r, _c);                                                                 \
            __builtin_amdgcn_global_load_lds((const unsigned*)(_g + (size_t)_r * K + _c),         \
                                             (unsigned*)((char*)(P) + _b), 16, 0, 0);             \
        }                                                                                         \
    } while (0)
#define STAGE_A(P, br, kt) STAGE_A_X(threadIdx.x, P, br, kt)
#define STAGE_B(P, W, kt)                                                                         \
    do {                                                                                          \
        const bf16* _g = (W) + (size_t)(kt) * 64 * LDB + ncol0;                                   \
        for (int _i = 0; _i < 2; ++_i) {                                                          \
            int _b = threadIdx.x * 16 + _i * 8192, _r = _b >> 8, _p = (_b >> 4) & 15;             \
            int _ch = _p ^ (((_r & 3) << 2) | ((_r >> 2) & 3));                                   \
            __builtin_amdgcn_global_load_lds((const unsigned*)(_g + (size_t)_r * LDB + _ch * 8),  \
                                             (unsigned*)((char*)(P) + _b), 16, 0, 0);             \
        }                                                                                         \
    } while (0)
#define LDA(dst, b, h)                                                                            \
    for (int m = 0; m < 4; ++m)                                                                   \
        for (int k = 0; k < 2; ++k)                                                               \
    dst[m][k] = *reinterpret_cast<const bf16x8*>((char*)SA(b, h) + lds_byte(wr * 64 + m * 16 + fr, k * 32 + fq * 8))
#define LDB_(dst, b, h)                                                                           \
    for (int n = 0; n < 2; ++n)                                                                   \
        for (int k = 0; k < 2; ++k) {                                                             \
            TR_READ(dst##_lo[n][k], tro[n][k][0], ((b) * 2 + (h)) * 16384);                         \
            TR_READ(dst##_hi[n][k], tro[n][k][1], ((b) * 2 + (h)) * 16384);                         \
        }
#define FIXB(dst)                                                                                 \
    for (int n = 0; n < 2; ++n)                                                                   \
        for (int k = 0; k < 2; ++k) {                                                             \
            PIN(dst##_lo[n][k]); PIN(dst##_hi[n][k]);                                                 \
            dst[n][k] = cat8(dst##_lo[n][k], dst##_hi[n][k]);                                         \
        }
#define MMA(ai, bj, At, Bt)                                                                       \
    do {                                                                                          \
        __builtin_amdgcn_s_setprio(1);                                                            \
        for (int m = 0; m < 4; ++m)                                                               \
            for (int n = 0; n < 2; ++n)                                                           \
                for (int k = 0; k < 2; ++k)                                                       \
                    acc[ai][bj][m][n] =                                                           \
                        __builtin_amdgcn_mfma_f32_16x16x32_bf16(Bt[n][k], At[m][k], acc[ai][bj][m][n], 0, 0, 0); \
        __builtin_amdgcn_s_setprio(0);                                                            \
    } while (0)

    const int nM = SEQ / 256;
    const int wgid = (blockIdx.x & 7) * 28 + (blockIdx.x >> 3);
    const int pm = wgid % nM, pn = wgid / nM;
    const int brow = pm * 256, ncol0 = pn * 128;
    int wid = threadIdx.x >> 6, lane = threadIdx.x & 63, wr = wid >> 2, wc = wid & 3, fr = lane & 15, fq = lane >> 4;
    f32x4 acc[2][2][4][2] = {};
    bf16x8 At[4][2], B0[2][2], B1[2][2];
    bf16x4 B0_lo[2][2], B0_hi[2][2], B1_lo[2][2], B1_hi[2][2];
    unsigned tro[2][2][2];
    for (int n = 0; n < 2; ++n)
        for (int k = 0; k < 2; ++k)
            for (int t = 0; t < 2; ++t) tro[n][k][t] = (unsigned)(size_t)shm + tr_off(wc * 2 + n, k, t, lane);
    constexpr int nt = K / 64;

    STAGE_B(SB(0, 0), Wg, 0); STAGE_A(SA(0, 0), brow, 0);
    STAGE_B(SB(0, 1), Wu, 0); STAGE_A(SA(0, 1), brow + 128, 0);
    if (wr == 1) BAR;
    WAIT_V(4); BAR;
    STAGE_B(SB(1, 0), Wg, 1); STAGE_A(SA(1, 0), brow, 1); STAGE_B(SB(1, 1), Wu, 1);
    WAIT_V(6); BAR;
    for (int t = 0; t < nt - 2; t += 2) {
        LDB_(B0, 0, 0); SCHED; LDA(At, 0, 0); STAGE_A(SA(1, 1), brow + 128, t + 1);
        WAIT_L(8); BAR; WAIT_L(0); FIXB(B0); MMA(0, 0, At, B0); BAR; SCHED;
        LDB_(B1, 0, 1); STAGE_B(SB(0, 0), Wg, t + 2);
        BAR; WAIT_L(0); FIXB(B1); MMA(0, 1, At, B1); BAR;
        LDA(At, 0, 1); STAGE_A(SA(0, 0), brow, t + 2);
        BAR; WAIT_L(0); MMA(1, 0, At, B0); BAR; SCHED;
        STAGE_B(SB(0, 1), Wu, t + 2);
        WAIT_V(6); BAR; MMA(1, 1, At, B1); BAR;
        LDB_(B0, 1, 0); SCHED; LDA(At, 1, 0); STAGE_A(SA(0, 1), brow + 128, t + 2);
        WAIT_L(8); BAR; WAIT_L(0); FIXB(B0); MMA(0, 0, At, B0); BAR; SCHED;
        LDB_(B1, 1, 1); STAGE_B(SB(1, 0), Wg, t + 3);
        BAR; WAIT_L(0); FIXB(B1); MMA(0, 1, At, B1); BAR;
        LDA(At, 1, 1); STAGE_A(SA(1, 0), brow, t + 3);
        BAR; WAIT_L(0); MMA(1, 0, At, B0); BAR; SCHED;
        STAGE_B(SB(1, 1), Wu, t + 3);
        WAIT_V(6); BAR; MMA(1, 1, At, B1); BAR;
    }
    int tz = threadIdx.x;
    asm volatile("" : "+v"(tz));
    wid = tz >> 6; lane = tz & 63; wr = wid >> 2; wc = wid & 3; fr = lane & 15; fq = lane >> 4;
    {
        LDB_(B0, 0, 0); WAIT_V(0); LDA(At, 0, 0); STAGE_A_X(tz, SA(1, 1), brow + 128, nt - 1);
        BAR; WAIT_L(0); FIXB(B0); MMA(0, 0, At, B0); BAR;
        LDB_(B1, 0, 1); BAR; WAIT_L(0); FIXB(B1); MMA(0, 1, At, B1); BAR;
        LDA(At, 0, 1); WAIT_V(4); BAR; WAIT_L(0); MMA(1, 0, At, B0); MMA(1, 1, At, B1); BAR;
    }
    {
        LDB_(B0, 1, 0); LDA(At, 1, 0); WAIT_V(2); BAR; WAIT_L(0); FIXB(B0); MMA(0, 0, At, B0); BAR;
        LDB_(B1, 1, 1); WAIT_V(0); BAR; WAIT_L(0); FIXB(B1); MMA(0, 1, At, B1); BAR;
        LDA(At, 1, 1); BAR; WAIT_L(0); MMA(1, 0, At, B0); MMA(1, 1, At, B1); BAR;
    }
    if (wr == 0) BAR;
    {
        char* tile = reinterpret_cast<char*>(shm);
#pragma unroll
        for (int ai = 0; ai < 2; ++ai)
#pragma unroll
            for (int m = 0; m < 4; ++m)
#pragma unroll
                for (int n = 0; n < 2; ++n) {
                    const int row = ai * 128 + wr * 64 + m * 16 + fr;
                    const int col = wc * 32 + n * 16 + fq * 4;
                    uint2 pk;
                    pk.x = geglu_pair(acc[ai][0][m][n][0], acc[ai][0][m][n][1], acc[ai][1][m][n][0], acc[ai][1][m][n][1]);
                    pk.y = geglu_pair(acc[ai][0][m][n][2], acc[ai][0][m][n][3], acc[ai][1][m][n][2], acc[ai][1][m][n][3]);
                    *reinterpret_cast<uint2*>(tile + row * 272 + col * 2) = pk;
                }
        __syncthreads();
#pragma unroll
        for (int i = 0; i < 8; ++i) {
            const int row = wid * 32 + i * 4 + (lane >> 4), c16 = lane & 15;
            const uint4 v = *reinterpret_cast<const uint4*>(tile + row * 272 + c16 * 16);
            *reinterpret_cast<uint4*>(H + (size_t)(brow + row) * DFF + ncol0 + c16 * 8) = v;
        }
    }
#undef SA
#undef SB
#undef STAGE_A_X
#undef STAGE_A
#undef STAGE_B
#undef LDA
#undef LDB_
#undef FIXB
#undef MMA
}

constexpr int G2_SHM = 4 * 32768;

__global__ __launch_bounds__(512, 2) void k_gemm2(const bf16* __restrict__ A, const bf16* __restrict__ W,
                                                  float* __restrict__ P0, float* __restrict__ P1) {
    extern __shared__ __attribute__((aligned(16))) char lds2[];
    constexpr int K = DFF, LDB = DM, KH = K / 2;
    const int wgid = blockIdx.x;
    const int sk = wgid & 1, tile = wgid >> 1;
    const int pm = tile & 15, pn = tile >> 4;
    const int brow = pm * 128, ncol0 = pn * 128, kbase = sk * KH;
    int tid = threadIdx.x, wid = tid >> 6, lane = tid & 63, wr = wid >> 2, wc = wid & 3, fr = lane & 15,
        fq = lane >> 4;
    f32x4 acc[4][2] = {};
    constexpr int nt = KH / 64;

    int ar[2], ac[2], brr[2], bch[2];
#pragma unroll
    for (int i = 0; i < 2; ++i) {
        int b = tid * 16 + i * 8192;
        stage_rc(b, ar[i], ac[i]);
        brr[i] = b >> 8;
        bch[i] = ((b >> 4) & 15) ^ (((brr[i] & 3) << 2) | ((brr[i] >> 2) & 3));
    }
    const bf16* ga = A + (size_t)brow * K + kbase;
    const bf16* gb = W + (size_t)kbase * LDB + ncol0;
#define G2_STAGE(buf, kt)                                                                              \
    do {                                                                                               \
        _Pragma("unroll") for (int i = 0; i < 2; ++i)                                                  \
            __builtin_amdgcn_global_load_lds((const unsigned*)(gb + (size_t)((kt) * 64 + brr[i]) * LDB + bch[i] * 8), \
                                             (unsigned*)(lds2 + (buf) * 16384 + tid * 16 + i * 8192), 16, 0, 0); \
        _Pragma("unroll") for (int i = 0; i < 2; ++i)                                                  \
            __builtin_amdgcn_global_load_lds((const unsigned*)(ga + (size_t)ar[i] * K + (kt) * 64 + ac[i]), \
                                             (unsigned*)(lds2 + 65536 + (buf) * 16384 + tid * 16 + i * 8192), 16, 0, 0); \
    } while (0)

    unsigned tro[2][2][2];
#pragma unroll
    for (int n = 0; n < 2; ++n)
#pragma unroll
        for (int k = 0; k < 2; ++k)
#pragma unroll
            for (int t = 0; t < 2; ++t) tro[n][k][t] = (unsigned)(size_t)lds2 + tr_off(wc * 2 + n, k, t, lane);

    bf16x8 At[4][2], Bt[2][2];
    bf16x4 Bl[2][2], Bh[2][2];
#define G2_STEP(buf, t, DO_STAGE, VW)                                                                  \
    do {                                                                                               \
        _Pragma("unroll") for (int n = 0; n < 2; ++n) _Pragma("unroll") for (int k = 0; k < 2; ++k) {  \
            TR_READ(Bl[n][k], tro[n][k][0], (buf) * 16384);                                            \
            TR_READ(Bh[n][k], tro[n][k][1], (buf) * 16384);                                            \
        }                                                                                              \
        _Pragma("unroll") for (int m = 0; m < 4; ++m) _Pragma("unroll") for (int k = 0; k < 2; ++k)    \
            At[m][k] = *reinterpret_cast<const bf16x8*>(lds2 + 65536 + (buf) * 16384 +                 \
                                                        lds_byte(wr * 64 + m * 16 + fr, k * 32 + fq * 8)); \
        if (DO_STAGE) G2_STAGE(((buf) + 3) & 3, (t) + 3);                                              \
        WAIT_L(0);                                                                                     \
        BAR;                                                                                           \
        _Pragma("unroll") for (int n = 0; n < 2; ++n) _Pragma("unroll") for (int k = 0; k < 2; ++k) {  \
            PIN(Bl[n][k]); PIN(Bh[n][k]);                                                              \
            Bt[n][k] = cat8(Bl[n][k], Bh[n][k]);                                                       \
        }                                                                                              \
        __builtin_amdgcn_s_setprio(1);                                                                 \
        _Pragma("unroll") for (int m = 0; m < 4; ++m) _Pragma("unroll") for (int n = 0; n < 2; ++n)    \
            _Pragma("unroll") for (int k = 0; k < 2; ++k)                                              \
                acc[m][n] = __builtin_amdgcn_mfma_f32_16x16x32_bf16(Bt[n][k], At[m][k], acc[m][n], 0, 0, 0); \
        __builtin_amdgcn_s_setprio(0);                                                                 \
        WAIT_V(VW);                                                                                    \
        BAR;                                                                                           \
    } while (0)

    G2_STAGE(0, 0);
    G2_STAGE(1, 1);
    G2_STAGE(2, 2);
    if (wr == 1) BAR;
    WAIT_V(4);
    BAR;
    BAR;
    for (int t = 0; t < nt - 4; t += 4) {
        G2_STEP(0, t, true, 4);
        G2_STEP(1, t + 1, true, 4);
        G2_STEP(2, t + 2, true, 4);
        G2_STEP(3, t + 3, true, 4);
    }
    G2_STEP(0, nt - 4, true, 4);
    G2_STEP(1, nt - 3, false, 0);
    G2_STEP(2, nt - 2, false, 0);
    G2_STEP(3, nt - 1, false, 0);
    if (wr == 0) BAR;
    float* P = sk ? P1 : P0;
#pragma unroll
    for (int m = 0; m < 4; ++m)
#pragma unroll
        for (int n = 0; n < 2; ++n) {
            const int row = brow + wr * 64 + m * 16 + fr;
            const int col = ncol0 + wc * 32 + n * 16 + fq * 4;
            *reinterpret_cast<f32x4*>(P + (size_t)row * DM + col) = acc[m][n];
        }
#undef G2_STAGE
#undef G2_STEP
}

extern "C" void kernel_launch(void* const* d_in, const int* in_sizes, int n_in, void* d_out, int out_size,
                              void* d_ws, size_t ws_size, hipStream_t stream) {
    const bf16* x = (const bf16*)d_in[0];
    const bf16* wre = (const bf16*)d_in[1];
    const bf16* wim = (const bf16*)d_in[2];
    const bf16* wg = (const bf16*)d_in[4];
    const bf16* wu = (const bf16*)d_in[5];
    const bf16* wd = (const bf16*)d_in[6];
    bf16* out = (bf16*)d_out;

    char* ws = (char*)d_ws;
    size_t off = 0;
    auto take = [&](size_t bytes) {
        char* p = ws + off;
        off += (bytes + 255) & ~(size_t)255;
        return p;
    };
    bf16* xn = (bf16*)take((size_t)SEQ * DM * 2);
    bf16* h = (bf16*)take((size_t)SEQ * DFF * 2);
    cf* Z1 = (cf*)take((size_t)SEQ * KP * 8);
    cf* Z2 = (cf*)take((size_t)SEQ * KP * 8);
    float* p0 = (float*)take((size_t)SEQ * DM * 4);
    float* p1 = (float*)take((size_t)SEQ * DM * 4);

    (void)hipFuncSetAttribute((const void*)k_gemm1, hipFuncAttributeMaxDynamicSharedMemorySize, G1_SHM);
    (void)hipFuncSetAttribute((const void*)k_gemm2, hipFuncAttributeMaxDynamicSharedMemorySize, G2_SHM);

    k_ln_fft_fwd<<<256, 512, 0, stream>>>(x, xn, Z1);
    k_fft_col<<<8 * 33, 256, 0, stream>>>(Z1, Z2, wre, wim);
    k_gemm1<<<256, 512, G1_SHM, stream>>>(xn, wg, wu, h, Z1, Z2, wd, wim);
    k_gemm2<<<256, 512, G2_SHM, stream>>>(h, wd, p0, p1);
    k_fft_inv_out<<<256, 512, 0, stream>>>(Z2, x, p0, p1, out);
}
```

```cpp
#include <hip/hip_runtime.h>
#include <hip/hip_bf16.h>
#include <stdint.h>

typedef __hip_bfloat16 bf16;
typedef short bf16x8 __attribute__((ext_vector_type(8)));
typedef short bf16x4 __attribute__((ext_vector_type(4)));
typedef float f32x4 __attribute__((ext_vector_type(4)));

#define DEV __device__ __forceinline__

constexpr int SEQ = 2048;
constexpr int DM = 1024;
constexpr int KF = 513;
constexpr int KP = 520;
constexpr int DFF = 3584;

struct cf { float x, y; };

DEV cf cadd(cf a, cf b) { return {a.x + b.x, a.y + b.y}; }
DEV cf csub(cf a, cf b) { return {a.x - b.x, a.y - b.y}; }
DEV cf cmul(cf a, cf b) { return {a.x * b.x - a.y * b.y, a.x * b.y + a.y * b.x}; }
DEV cf cmulc(cf a, cf b) { return {a.x * b.x + a.y * b.y, a.y * b.x - a.x * b.y}; }
DEV cf cconj(cf a) { return {a.x, -a.y}; }

DEV float bfr(float x) {
    unsigned u = __float_as_uint(x);
    u += 0x7fffu + ((u >> 16) & 1u);
    return __uint_as_float(u & 0xffff0000u);
}
DEV unsigned short bf_bits(float x) {
    unsigned u = __float_as_uint(x);
    u += 0x7fffu + ((u >> 16) & 1u);
    return (unsigned short)(u >> 16);
}
DEV float bf_lo(unsigned v) { return __uint_as_float(v << 16); }
DEV float bf_hi(unsigned v) { return __uint_as_float(v & 0xffff0000u); }
DEV float bf2f(bf16 v) { return __uint_as_float(((unsigned)*reinterpret_cast<const unsigned short*>(&v)) << 16); }

DEV constexpr float c16(int j) {
    return j == 0 ? 1.0f : j == 1 ? 0.92387953251128674f : j == 2 ? 0.70710678118654752f : j == 3 ? 0.38268343236508977f
         : j == 4 ? 0.0f : j == 5 ? -0.38268343236508977f : j == 6 ? -0.70710678118654752f : -0.92387953251128674f;
}
DEV constexpr float s16(int j) {
    return j == 0 ? 0.0f : j == 1 ? 0.38268343236508977f : j == 2 ? 0.70710678118654752f : j == 3 ? 0.92387953251128674f
         : j == 4 ? 1.0f : j == 5 ? 0.92387953251128674f : j == 6 ? 0.70710678118654752f : 0.38268343236508977f;
}

template <int N, int SG>
struct Dft {
    static DEV void run(cf* v) {
        cf a[N / 2], b[N / 2];
#pragma unroll
        for (int j = 0; j < N / 2; ++j) {
            a[j] = cadd(v[j], v[j + N / 2]);
            cf d = csub(v[j], v[j + N / 2]);
            const int t = j * (16 / N);
            if (t == 0) {
                b[j] = d;
            } else if (t == 4) {
                b[j] = SG < 0 ? cf{d.y, -d.x} : cf{-d.y, d.x};
            } else {
                const float cr = c16(t), si = SG * s16(t);
                b[j] = {d.x * cr - d.y * si, d.x * si + d.y * cr};
            }
        }
        Dft<N / 2, SG>::run(a);
        Dft<N / 2, SG>::run(b);
#pragma unroll
        for (int r = 0; r < N / 2; ++r) {
            v[2 * r] = a[r];
            v[2 * r + 1] = b[r];
        }
    }
};
template <int SG>
struct Dft<1, SG> {
    static DEV void run(cf*) {}
};

DEV void build_tw2048(cf* tw, int tid, int nthreads) {
    for (int j = tid; j < 2048; j += nthreads) {
        float s, c;
        sincospif((float)j * (1.0f / 1024.0f), &s, &c);
        tw[j] = {c, -s};
    }
}
template <int SG>
DEV cf twv(const cf* tw, int j) {
    cf t = tw[j];
    return SG < 0 ? t : cf{t.x, -t.y};
}

constexpr int RLEN = 576;

template <int SG>
DEV void fft512_wave(cf (&v)[8], cf* ex, const cf* tw, int lane) {
    Dft<8, SG>::run(v);
#pragma unroll
    for (int r = 1; r < 8; ++r) v[r] = cmul(v[r], twv<SG>(tw, 4 * lane * r));
#pragma unroll
    for (int r = 0; r < 8; ++r) ex[r * 68 + lane] = v[r];
    __builtin_amdgcn_wave_barrier();
    const int l0 = lane & 7, rr = lane >> 3;
#pragma unroll
    for (int l1 = 0; l1 < 8; ++l1) v[l1] = ex[rr * 68 + l0 + 8 * l1];
    __builtin_amdgcn_wave_barrier();
    Dft<8, SG>::run(v);
#pragma unroll
    for (int q1 = 1; q1 < 8; ++q1) v[q1] = cmul(v[q1], twv<SG>(tw, 32 * l0 * q1));
#pragma unroll
    for (int q1 = 0; q1 < 8; ++q1) ex[q1 * 66 + lane] = v[q1];
    __builtin_amdgcn_wave_barrier();
    const int r2 = lane & 7, q1s = lane >> 3;
#pragma unroll
    for (int l0i = 0; l0i < 8; ++l0i) v[l0i] = ex[q1s * 66 + l0i + 8 * r2];
    __builtin_amdgcn_wave_barrier();
    Dft<8, SG>::run(v);
}

__global__ __launch_bounds__(576) void k_ln_fft_fwd(const bf16* __restrict__ x, bf16* __restrict__ xn,
                                                    cf* __restrict__ Z1) {
    __shared__ cf tw[2048];
    __shared__ cf R[8][RLEN];
    const int tid = threadIdx.x, lane = tid & 63, b = tid >> 6;
    const int a = blockIdx.x;
    if (b < 8) {
    const int row = a + 256 * b;

    const unsigned* xr = reinterpret_cast<const unsigned*>(x + (size_t)row * DM);
    unsigned raw[8];
#pragma unroll
    for (int j = 0; j < 8; ++j) raw[j] = xr[lane + 64 * j];

    build_tw2048(tw, tid, 512);

    float sum = 0.f;
#pragma unroll
    for (int j = 0; j < 8; ++j) sum += bf_lo(raw[j]) + bf_hi(raw[j]);
#pragma unroll
    for (int o = 32; o > 0; o >>= 1) sum += __shfl_xor(sum, o);
    const float mean = sum * (1.0f / DM);
    float sq = 0.f;
#pragma unroll
    for (int j = 0; j < 8; ++j) {
        float d0 = bf_lo(raw[j]) - mean, d1 = bf_hi(raw[j]) - mean;
        sq += d0 * d0 + d1 * d1;
    }
#pragma unroll
    for (int o = 32; o > 0; o >>= 1) sq += __shfl_xor(sq, o);
    const float mb = bfr(mean);
    const float vb = bfr(sq * (1.0f / DM));
    const float den = bfr(sqrtf(bfr(vb + 1e-5f)));

    cf v[8];
    unsigned* xo = reinterpret_cast<unsigned*>(xn + (size_t)row * DM);
#pragma unroll
    for (int j = 0; j < 8; ++j) {
        float e0 = bfr(bfr(bf_lo(raw[j]) - mb) / den);
        float e1 = bfr(bfr(bf_hi(raw[j]) - mb) / den);
        v[j] = {e0, e1};
        xo[lane + 64 * j] = (__float_as_uint(e0) >> 16) | (__float_as_uint(e1) & 0xffff0000u);
    }
    __syncthreads();

    fft512_wave<-1>(v, R[b], tw, lane);
#pragma unroll
    for (int j = 0; j < 8; ++j) R[b][lane + 64 * j] = v[j];
    __syncthreads();
    } else {
        __syncthreads();
        __syncthreads();
    }

    if (tid < KF) {
        const int k = tid;
        const int k1 = k & 511, k2 = (512 - k) & 511;
        const cf w = tw[2 * k];
        cf u[8];
#pragma unroll
        for (int bb = 0; bb < 8; ++bb) {
            cf p = R[bb][k1], q = cconj(R[bb][k2]);
            cf e = {0.5f * (p.x + q.x), 0.5f * (p.y + q.y)};
            cf o = {0.5f * (p.x - q.x), 0.5f * (p.y - q.y)};
            cf wo = cmul(w, o);
            u[bb] = {e.x + wo.y, e.y - wo.x};
        }
        Dft<8, -1>::run(u);
#pragma unroll
        for (int bp = 0; bp < 8; ++bp) {
            cf o = bp ? cmul(u[bp], tw[a * bp]) : u[bp];
            Z1[(size_t)(a * 8 + bp) * KP + k] = o;
        }
    }
}

constexpr int CT_LD = 17;
DEV void fft_col_tile(const cf* __restrict__ Z1, cf* __restrict__ Z2, const bf16* __restrict__ wr,
                      const bf16* __restrict__ wi, cf* T, const cf* tw, int bp, int kt, int t) {
    const int k0 = kt * 16;
    const int kk = t & 15, j = t >> 4;
    const int kcl = (k0 + kk < KF) ? (k0 + kk) : 0;
    unsigned short wq0[16], wq1[16];
#pragma unroll
    for (int c = 0; c < 16; ++c) {
        const int sp = 8 * (j + 16 * c) + bp;
        wq0[c] = *reinterpret_cast<const unsigned short*>(wr + (size_t)sp * KF + kcl);
        wq1[c] = *reinterpret_cast<const unsigned short*>(wi + (size_t)sp * KF + kcl);
    }
#pragma unroll
    for (int i = 0; i < 8; ++i) {
        const int a = (t >> 3) + 32 * i, c = t & 7;
        float4 g = float4{0.f, 0.f, 0.f, 0.f};
        if (k0 + 2 * c < KP) g = *reinterpret_cast<const float4*>(Z1 + (size_t)(a * 8 + bp) * KP + k0 + 2 * c);
        T[a * CT_LD + 2 * c] = {g.x, g.y};
        T[a * CT_LD + 2 * c + 1] = {g.z, g.w};
    }
    __syncthreads();
    cf v[16];
#pragma unroll
    for (int i = 0; i < 16; ++i) v[i] = T[(j + 16 * i) * CT_LD + kk];
    Dft<16, -1>::run(v);
#pragma unroll
    for (int d = 1; d < 16; ++d) v[d] = cmul(v[d], tw[j * d]);
#pragma unroll
    for (int d = 0; d < 16; ++d) T[(j + 16 * d) * CT_LD + kk] = v[d];
    __syncthreads();
    const int dd = j;
#pragma unroll
    for (int i = 0; i < 16; ++i) v[i] = T[(i + 16 * dd) * CT_LD + kk];
    Dft<16, -1>::run(v);
#pragma unroll
    for (int c = 0; c < 16; ++c)
        v[c] = cmul(v[c], cf{__uint_as_float((unsigned)wq0[c] << 16), __uint_as_float((unsigned)wq1[c] << 16)});
    Dft<16, 1>::run(v);
#pragma unroll
    for (int i = 1; i < 16; ++i) v[i] = cmulc(v[i], tw[i * dd]);
#pragma unroll
    for (int i = 0; i < 16; ++i) T[(i + 16 * dd) * CT_LD + kk] = v[i];
    __syncthreads();
#pragma unroll
    for (int d = 0; d < 16; ++d) v[d] = T[(j + 16 * d) * CT_LD + kk];
    Dft<16, 1>::run(v);
#pragma unroll
    for (int i = 0; i < 16; ++i) T[(j + 16 * i) * CT_LD + kk] = v[i];
    __syncthreads();
#pragma unroll
    for (int i = 0; i < 8; ++i) {
        const int a = (t >> 3) + 32 * i, c = t & 7;
        const cf p = T[a * CT_LD + 2 * c], q = T[a * CT_LD + 2 * c + 1];
        if (k0 + 2 * c < KP)
            *reinterpret_cast<float4*>(Z2 + (size_t)(a * 8 + bp) * KP + k0 + 2 * c) = float4{p.x, p.y, q.x, q.y};
    }
    __syncthreads();
}

__global__ __launch_bounds__(256) void k_fft_col(const cf* __restrict__ Z1, cf* __restrict__ Z2,
                                                 const bf16* __restrict__ wr, const bf16* __restrict__ wi) {
    __shared__ cf T[256 * CT_LD];
    __shared__ cf tw[256];
    const int t = threadIdx.x;
    {
        float s, c;
        sincospif((float)t * (1.0f / 128.0f), &s, &c);
        tw[t] = {c, -s};
    }
    const int bp = blockIdx.x & 7, kt = blockIdx.x >> 3;
    fft_col_tile(Z1, Z2, wr, wi, T, tw, bp, kt, t);
}

__global__ __launch_bounds__(576) void k_fft_inv_out(const cf* __restrict__ Z2, const bf16* __restrict__ x,
                                                     const float* __restrict__ p0, const float* __restrict__ p1,
                                                     bf16* __restrict__ out) {
    __shared__ cf tw[2048];
    __shared__ cf R[8][RLEN];
    const int tid = threadIdx.x, lane = tid & 63, b = tid >> 6;
    const int a = blockIdx.x;
    const int row = a + 256 * (b & 7);
    const unsigned* xr = reinterpret_cast<const unsigned*>(x + (size_t)row * DM);
    const float2* q0 = reinterpret_cast<const float2*>(p0 + (size_t)row * DM);
    const float2* q1 = reinterpret_cast<const float2*>(p1 + (size_t)row * DM);
    unsigned xpre[8];
    float2 a0p[8], a1p[8];
    cf zpre[8];
    if (tid < KF) {
#pragma unroll
        for (int bp = 0; bp < 8; ++bp) zpre[bp] = Z2[(size_t)(a * 8 + bp) * KP + tid];
    }
    if (b < 8) {
#pragma unroll
        for (int j = 0; j < 8; ++j) {
            xpre[j] = xr[lane + 64 * j];
            a0p[j] = q0[lane + 64 * j];
            a1p[j] = q1[lane + 64 * j];
        }
        build_tw2048(tw, tid, 512);
    }
    __syncthreads();
    if (tid < KF) {
        const int k = tid;
        cf u[8];
#pragma unroll
        for (int bp = 0; bp < 8; ++bp) {
            u[bp] = bp ? cmulc(zpre[bp], tw[a * bp]) : zpre[bp];
        }
        Dft<8, 1>::run(u);
#pragma unroll
        for (int bb = 0; bb < 8; ++bb) R[bb][k] = u[bb];
    }
    __syncthreads();
    if (b >= 8) return;

    cf v[8];
#pragma unroll
    for (int j = 0; j < 8; ++j) {
        const int k = lane + 64 * j;
        cf p = R[b][k], q = R[b][512 - k];
        if (k == 0) { p.y = 0.f; q.y = 0.f; }
        q = cconj(q);
        cf e = cadd(p, q);
        cf o = cmulc(csub(p, q), tw[2 * k]);
        v[j] = {e.x - o.y, e.y + o.x};
    }
    __builtin_amdgcn_wave_barrier();
    fft512_wave<1>(v, R[b], tw, lane);

    unsigned* orow = reinterpret_cast<unsigned*>(out + (size_t)row * DM);
    const float sc = 1.0f / (2048.0f * 1024.0f);
#pragma unroll
    for (int j = 0; j < 8; ++j) {
        const int n = lane + 64 * j;
        const unsigned xv = xpre[j];
        const float2 a0 = a0p[j], a1 = a1p[j];
        const float d0 = bfr(a0.x + a1.x), d1 = bfr(a0.y + a1.y);
        const float g0 = bfr(d0 / (1.0f + __expf(-d0))), g1 = bfr(d1 / (1.0f + __expf(-d1)));
        const float s0 = bfr(v[j].x * sc), s1 = bfr(v[j].y * sc);
        const float o0 = bfr(bf_lo(xv) + bfr(s0 * g0)), o1 = bfr(bf_hi(xv) + bfr(s1 * g1));
        orow[n] = (__float_as_uint(o0) >> 16) | (__float_as_uint(o1) & 0xffff0000u);
    }
}

DEV int lds_byte(int r, int c) {
    int st = (r >> 4) * 2 + (c >> 5), rr = r & 15, cc = c & 31, ob = rr * 64 + cc * 2;
    return st * 1024 + (ob ^ (((ob >> 9) & 1) << 5));
}
DEV void stage_rc(int b, int& R, int& C) {
    int st = b / 1024, sb = b % 1024, swz = sb ^ (((sb >> 9) & 1) << 5);
    R = (st >> 1) * 16 + swz / 64;
    C = (st & 1) * 32 + (swz % 64) / 2;
}
DEV int b_off(int row, int ch) { return 256 * row + 16 * (ch ^ (((row & 3) << 2) | ((row >> 2) & 3))); }

DEV unsigned tr_off(int c, int ks, int t, int lane) {
    const int g = lane >> 4, q = (lane & 15) >> 2, p = lane & 3;
    return (unsigned)(b_off(32 * ks + 8 * g + 4 * t + q, 2 * c + (p >> 1)) + 8 * (p & 1));
}
#define TR_READ(dst, addr, OFF) \
    asm volatile("ds_read_b64_tr_b16 %0, %1 offset:%2" : "=v"(dst) : "v"(addr), "i"(OFF))
#define PIN(x) asm volatile("" : "+v"(x))
DEV bf16x8 cat8(bf16x4 lo, bf16x4 hi) {
    bf16x8 r;
    r[0] = lo[0]; r[1] = lo[1]; r[2] = lo[2]; r[3] = lo[3];
    r[4] = hi[0]; r[5] = hi[1]; r[6] = hi[2]; r[7] = hi[3];
    return r;
}

DEV float gelu_tanh(float x) {
    const float u2 = 1.5957691216057308f * (x + 0.044715f * x * x * x);
    return x * __frcp_rn(1.0f + __expf(-u2));
}
DEV unsigned pack_bf16(float a, float b);
DEV unsigned geglu_pair(float g0, float g1, float u0, float u1) {
    const unsigned gp = pack_bf16(g0, g1), up = pack_bf16(u0, u1);
    const float a0 = bf_lo(gp), a1 = bf_hi(gp);
    const float c1 = -1.5957691216057308f * 1.4426950408889634f, c3 = c1 * 0.044715f;
    const float e0 = __builtin_amdgcn_exp2f(a0 * fmaf(a0 * a0, c3, c1));
    const float e1 = __builtin_amdgcn_exp2f(a1 * fmaf(a1 * a1, c3, c1));
    const float t0 = a0 * __builtin_amdgcn_rcpf(1.0f + e0), t1 = a1 * __builtin_amdgcn_rcpf(1.0f + e1);
    const unsigned tp = pack_bf16(t0, t1);
    return pack_bf16(bf_lo(tp) * bf_lo(up), bf_hi(tp) * bf_hi(up));
}
DEV unsigned pack_bf16(float a, float b) {
    unsigned r;
    asm("v_cvt_pk_bf16_f32 %0, %1, %2" : "=v"(r) : "v"(a), "v"(b));
    return r;
}

#define WAIT_V(n) asm volatile("s_waitcnt vmcnt(" #n ")" ::: "memory")
#define WAIT_L(n) asm volatile("s_waitcnt lgkmcnt(" #n ")" ::: "memory")
#define BAR __builtin_amdgcn_s_barrier()
#define SCHED __builtin_amdgcn_sched_barrier(0)

constexpr int G1_HT = 128 * 64;
constexpr int G1_SHM = 8 * G1_HT * 2;

__global__ __launch_bounds__(512, 2) void k_gemm1(const bf16* __restrict__ A, const bf16* __restrict__ Wg,
                                                  const bf16* __restrict__ Wu, bf16* __restrict__ H,
                                                  const cf* __restrict__ Z1, cf* __restrict__ Z2,
                                                  const bf16* __restrict__ wre, const bf16* __restrict__ wim) {
    extern __shared__ __attribute__((aligned(16))) bf16 shm[];
    if (blockIdx.x >= 224) {
        cf* base = reinterpret_cast<cf*>(shm);
        cf* tw = base + 2 * 256 * CT_LD;
        const int tid = threadIdx.x, half = tid >> 8, t = tid & 255;
        if (tid < 256) {
            float sn, cs;
            sincospif((float)tid * (1.0f / 128.0f), &sn, &cs);
            tw[tid] = {cs, -sn};
        }
        for (int pr = blockIdx.x - 224; pr < 132; pr += 32) {
            const int tile = pr * 2 + half;
            fft_col_tile(Z1, Z2, wre, wim, base + half * 256 * CT_LD, tw, tile & 7, tile >> 3, t);
        }
        return;
    }
    constexpr int K = DM, LDB = DFF;
#define SA(b, h) (shm + (4 + (b) * 2 + (h)) * G1_HT)
#define SB(b, h) (shm + ((b) * 2 + (h)) * G1_HT)
#define STAGE_A_X(T, P, br, kt)                                                                   \
    do {                                                                                          \
        const bf16* _g = A + (size_t)(br) * K + (kt) * 64;                                        \
        for (int _i = 0; _i < 2; ++_i) {                                                          \
            int _b = (T) * 16 + _i * 8192, _r, _c;                                                \
            stage_rc(_b, _r, _c);                                                                 \
            __builtin_amdgcn_global_load_lds((const unsigned*)(_g + (size_t)_r * K + _c),         \
                                             (unsigned*)((char*)(P) + _b), 16, 0, 0);             \
        }                                                                                         \
    } while (0)
#define STAGE_A(P, br, kt) STAGE_A_X(threadIdx.x, P, br, kt)
#define STAGE_B(P, W, kt)                                                                         \
    do {                                                                                          \
        const bf16* _g = (W) + (size_t)(kt) * 64 * LDB + ncol0;                                   \
        for (int _i = 0; _i < 2; ++_i) {                                                          \
            int _b = threadIdx.x * 16 + _i * 8192, _r = _b >> 8, _p = (_b >> 4) & 15;             \
            int _ch = _p ^ (((_r & 3) << 2) | ((_r >> 2) & 3));                                   \
            __builtin_amdgcn_global_load_lds((const unsigned*)(_g + (size_t)_r * LDB + _ch * 8),  \
                                             (unsigned*)((char*)(P) + _b), 16, 0, 0);             \
        }                                                                                         \
    } while (0)
#define LDA(dst, b, h)                                                                            \
    for (int m = 0; m < 4; ++m)                                                                   \
        for (int k = 0; k < 2; ++k)                                                               \
    dst[m][k] = *reinterpret_cast<const bf16x8*>((char*)SA(b, h) + lds_byte(wr * 64 + m * 16 + fr, k * 32 + fq * 8))
#define LDB_(dst, b, h)                                                                           \
    for (int n = 0; n < 2; ++n)                                                                   \
        for (int k = 0; k < 2; ++k) {                                                             \
            TR_READ(dst##_lo[n][k], tro[n][k][0], ((b) * 2 + (h)) * 16384);                         \
            TR_READ(dst##_hi[n][k], tro[n][k][1], ((b) * 2 + (h)) * 16384);                         \
        }
#define FIXB(dst)                                                                                 \
    for (int n = 0; n < 2; ++n)                                                                   \
        for (int k = 0; k < 2; ++k) {                                                             \
            PIN(dst##_lo[n][k]); PIN(dst##_hi[n][k]);                                                 \
            dst[n][k] = cat8(dst##_lo[n][k], dst##_hi[n][k]);                                         \
        }
#define MMA(ai, bj, At, Bt)                                                                       \
    do {                                                                                          \
        __builtin_amdgcn_s_setprio(1);                                                            \
        for (int m = 0; m < 4; ++m)                                                               \
            for (int n = 0; n < 2; ++n)                                                           \
                for (int k = 0; k < 2; ++k)                                                       \
                    acc[ai][bj][m][n] =                                                           \
                        __builtin_amdgcn_mfma_f32_16x16x32_bf16(Bt[n][k], At[m][k], acc[ai][bj][m][n], 0, 0, 0); \
        __builtin_amdgcn_s_setprio(0);                                                            \
    } while (0)

    const int nM = SEQ / 256;
    const int wgid = (blockIdx.x & 7) * 28 + (blockIdx.x >> 3);
    const int pm = wgid % nM, pn = wgid / nM;
    const int brow = pm * 256, ncol0 = pn * 128;
    int wid = threadIdx.x >> 6, lane = threadIdx.x & 63, wr = wid >> 2, wc = wid & 3, fr = lane & 15, fq = lane >> 4;
    f32x4 acc[2][2][4][2] = {};
    bf16x8 At[4][2], B0[2][2], B1[2][2];
    bf16x4 B0_lo[2][2], B0_hi[2][2], B1_lo[2][2], B1_hi[2][2];
    unsigned tro[2][2][2];
    for (int n = 0; n < 2; ++n)
        for (int k = 0; k < 2; ++k)
            for (int t = 0; t < 2; ++t) tro[n][k][t] = (unsigned)(size_t)shm + tr_off(wc * 2 + n, k, t, lane);
    constexpr int nt = K / 64;

    STAGE_B(SB(0, 0), Wg, 0); STAGE_A(SA(0, 0), brow, 0);
    STAGE_B(SB(0, 1), Wu, 0); STAGE_A(SA(0, 1), brow + 128, 0);
    if (wr == 1) BAR;
    WAIT_V(4); BAR;
    STAGE_B(SB(1, 0), Wg, 1); STAGE_A(SA(1, 0), brow, 1); STAGE_B(SB(1, 1), Wu, 1);
    WAIT_V(6); BAR;
    for (int t = 0; t < nt - 2; t += 2) {
        LDB_(B0, 0, 0); SCHED; LDA(At, 0, 0); STAGE_A(SA(1, 1), brow + 128, t + 1);
        WAIT_L(8); BAR; WAIT_L(0); FIXB(B0); MMA(0, 0, At, B0); BAR; SCHED;
        LDB_(B1, 0, 1); STAGE_B(SB(0, 0), Wg, t + 2);
        BAR; WAIT_L(0); FIXB(B1); MMA(0, 1, At, B1); BAR;
        LDA(At, 0, 1); STAGE_A(SA(0, 0), brow, t + 2);
        BAR; WAIT_L(0); MMA(1, 0, At, B0); BAR; SCHED;
        STAGE_B(SB(0, 1), Wu, t + 2);
        WAIT_V(6); BAR; MMA(1, 1, At, B1); BAR;
        LDB_(B0, 1, 0); SCHED; LDA(At, 1, 0); STAGE_A(SA(0, 1), brow + 128, t + 2);
        WAIT_L(8); BAR; WAIT_L(0); FIXB(B0); MMA(0, 0, At, B0); BAR; SCHED;
        LDB_(B1, 1, 1); STAGE_B(SB(1, 0), Wg, t + 3);
        BAR; WAIT_L(0); FIXB(B1); MMA(0, 1, At, B1); BAR;
        LDA(At, 1, 1); STAGE_A(SA(1, 0), brow, t + 3);
        BAR; WAIT_L(0); MMA(1, 0, At, B0); BAR; SCHED;
        STAGE_B(SB(1, 1), Wu, t + 3);
        WAIT_V(6); BAR; MMA(1, 1, At, B1); BAR;
    }
    int tz = threadIdx.x;
    asm volatile("" : "+v"(tz));
    wid = tz >> 6; lane = tz & 63; wr = wid >> 2; wc = wid & 3; fr = lane & 15; fq = lane >> 4;
    {
        LDB_(B0, 0, 0); WAIT_V(0); LDA(At, 0, 0); STAGE_A_X(tz, SA(1, 1), brow + 128, nt - 1);
        BAR; WAIT_L(0); FIXB(B0); MMA(0, 0, At, B0); BAR;
        LDB_(B1, 0, 1); BAR; WAIT_L(0); FIXB(B1); MMA(0, 1, At, B1); BAR;
        LDA(At, 0, 1); WAIT_V(4); BAR; WAIT_L(0); MMA(1, 0, At, B0); MMA(1, 1, At, B1); BAR;
    }
    {
        LDB_(B0, 1, 0); LDA(At, 1, 0); WAIT_V(2); BAR; WAIT_L(0); FIXB(B0); MMA(0, 0, At, B0); BAR;
        LDB_(B1, 1, 1); WAIT_V(0); BAR; WAIT_L(0); FIXB(B1); MMA(0, 1, At, B1); BAR;
        LDA(At, 1, 1); BAR; WAIT_L(0); MMA(1, 0, At, B0); MMA(1, 1, At, B1); BAR;
    }
    if (wr == 0) BAR;
    {
        char* tile = reinterpret_cast<char*>(shm);
#pragma unroll
        for (int ai = 0; ai < 2; ++ai)
#pragma unroll
            for (int m = 0; m < 4; ++m)
#pragma unroll
                for (int n = 0; n < 2; ++n) {
                    const int row = ai * 128 + wr * 64 + m * 16 + fr;
                    const int col = wc * 32 + n * 16 + fq * 4;
                    uint2 pk;
                    pk.x = geglu_pair(acc[ai][0][m][n][0], acc[ai][0][m][n][1], acc[ai][1][m][n][0], acc[ai][1][m][n][1]);
                    pk.y = geglu_pair(acc[ai][0][m][n][2], acc[ai][0][m][n][3], acc[ai][1][m][n][2], acc[ai][1][m][n][3]);
                    *reinterpret_cast<uint2*>(tile + row * 272 + col * 2) = pk;
                }
        __syncthreads();
#pragma unroll
        for (int i = 0; i < 8; ++i) {
            const int row = wid * 32 + i * 4 + (lane >> 4), c16 = lane & 15;
            const uint4 v = *reinterpret_cast<const uint4*>(tile + row * 272 + c16 * 16);
            *reinterpret_cast<uint4*>(H + (size_t)(brow + row) * DFF + ncol0 + c16 * 8) = v;
        }
    }
#undef SA
#undef SB
#undef STAGE_A_X
#undef STAGE_A
#undef STAGE_B
#undef LDA
#undef LDB_
#undef FIXB
#undef MMA
}

constexpr int G2_SHM = 4 * 32768;

__global__ __launch_bounds__(512, 2) void k_gemm2(const bf16* __restrict__ A, const bf16* __restrict__ W,
                                                  float* __restrict__ P0, float* __restrict__ P1) {
    extern __shared__ __attribute__((aligned(16))) char lds2[];
    constexpr int K = DFF, LDB = DM, KH = K / 2;
    const int wgid = blockIdx.x;
    const int sk = wgid & 1, tile = wgid >> 1;
    const int pm = tile & 15, pn = tile >> 4;
    const int brow = pm * 128, ncol0 = pn * 128, kbase = sk * KH;
    int tid = threadIdx.x, wid = tid >> 6, lane = tid & 63, wr = wid >> 2, wc = wid & 3, fr = lane & 15,
        fq = lane >> 4;
    f32x4 acc[4][2] = {};
    constexpr int nt = KH / 64;

    int ar[2], ac[2], brr[2], bch[2];
#pragma unroll
    for (int i = 0; i < 2; ++i) {
        int b = tid * 16 + i * 8192;
        stage_rc(b, ar[i], ac[i]);
        brr[i] = b >> 8;
        bch[i] = ((b >> 4) & 15) ^ (((brr[i] & 3) << 2) | ((brr[i] >> 2) & 3));
    }
    const bf16* ga = A + (size_t)brow * K + kbase;
    const bf16* gb = W + (size_t)kbase * LDB + ncol0;
#define G2_STAGE(buf, kt)                                                                              \
    do {                                                                                               \
        _Pragma("unroll") for (int i = 0; i < 2; ++i)                                                  \
            __builtin_amdgcn_global_load_lds((const unsigned*)(gb + (size_t)((kt) * 64 + brr[i]) * LDB + bch[i] * 8), \
                                             (unsigned*)(lds2 + (buf) * 16384 + tid * 16 + i * 8192), 16, 0, 0); \
        _Pragma("unroll") for (int i = 0; i < 2; ++i)                                                  \
            __builtin_amdgcn_global_load_lds((const unsigned*)(ga + (size_t)ar[i] * K + (kt) * 64 + ac[i]), \
                                             (unsigned*)(lds2 + 65536 + (buf) * 16384 + tid * 16 + i * 8192), 16, 0, 0); \
    } while (0)

    unsigned tro[2][2][2];
#pragma unroll
    for (int n = 0; n < 2; ++n)
#pragma unroll
        for (int k = 0; k < 2; ++k)
#pragma unroll
            for (int t = 0; t < 2; ++t) tro[n][k][t] = (unsigned)(size_t)lds2 + tr_off(wc * 2 + n, k, t, lane);

    bf16x8 At[4][2], Bt[2][2];
    bf16x4 Bl[2][2], Bh[2][2];
#define G2_STEP(buf, t, DO_STAGE, VW)                                                                  \
    do {                                                                                               \
        _Pragma("unroll") for (int n = 0; n < 2; ++n) _Pragma("unroll") for (int k = 0; k < 2; ++k) {  \
            TR_READ(Bl[n][k], tro[n][k][0], (buf) * 16384);                                            \
            TR_READ(Bh[n][k], tro[n][k][1], (buf) * 16384);                                            \
        }                                                                                              \
        _Pragma("unroll") for (int m = 0; m < 4; ++m) _Pragma("unroll") for (int k = 0; k < 2; ++k)    \
            At[m][k] = *reinterpret_cast<const bf16x8*>(lds2 + 65536 + (buf) * 16384 +                 \
                                                        lds_byte(wr * 64 + m * 16 + fr, k * 32 + fq * 8)); \
        if (DO_STAGE) G2_STAGE(((buf) + 3) & 3, (t) + 3);                                              \
        WAIT_L(0);                                                                                     \
        BAR;                                                                                           \
        _Pragma("unroll") for (int n = 0; n < 2; ++n) _Pragma("unroll") for (int k = 0; k < 2; ++k) {  \
            PIN(Bl[n][k]); PIN(Bh[n][k]);                                                              \
            Bt[n][k] = cat8(Bl[n][k], Bh[n][k]);                                                       \
        }                                                                                              \
        __builtin_amdgcn_s_setprio(1);                                                                 \
        _Pragma("unroll") for (int m = 0; m < 4; ++m) _Pragma("unroll") for (int n = 0; n < 2; ++n)    \
            _Pragma("unroll") for (int k = 0; k < 2; ++k)                                              \
                acc[m][n] = __builtin_amdgcn_mfma_f32_16x16x32_bf16(Bt[n][k], At[m][k], acc[m][n], 0, 0, 0); \
        __builtin_amdgcn_s_setprio(0);                                                                 \
        WAIT_V(VW);                                                                                    \
        BAR;                                                                                           \
    } while (0)

    G2_STAGE(0, 0);
    G2_STAGE(1, 1);
    G2_STAGE(2, 2);
    if (wr == 1) BAR;
    WAIT_V(4);
    BAR;
    BAR;
    for (int t = 0; t < nt - 4; t += 4) {
        G2_STEP(0, t, true, 4);
        G2_STEP(1, t + 1, true, 4);
        G2_STEP(2, t + 2, true, 4);
        G2_STEP(3, t + 3, true, 4);
    }
    G2_STEP(0, nt - 4, true, 4);
    G2_STEP(1, nt - 3, false, 0);
    G2_STEP(2, nt - 2, false, 0);
    G2_STEP(3, nt - 1, false, 0);
    if (wr == 0) BAR;
    float* P = sk ? P1 : P0;
#pragma unroll
    for (int m = 0; m < 4; ++m)
#pragma unroll
        for (int n = 0; n < 2; ++n) {
            const int row = brow + wr * 64 + m * 16 + fr;
            const int col = ncol0 + wc * 32 + n * 16 + fq * 4;
            *reinterpret_cast<f32x4*>(P + (size_t)row * DM + col) = acc[m][n];
        }
#undef G2_STAGE
#undef G2_STEP
}

extern "C" void kernel_launch(void* const* d_in, const int* in_sizes, int n_in, void* d_out, int out_size,
                              void* d_ws, size_t ws_size, hipStream_t stream) {
    const bf16* x = (const bf16*)d_in[0];
    const bf16* wre = (const bf16*)d_in[1];
    const bf16* wim = (const bf16*)d_in[2];
    const bf16* wg = (const bf16*)d_in[4];
    const bf16* wu = (const bf16*)d_in[5];
    const bf16* wd = (const bf16*)d_in[6];
    bf16* out = (bf16*)d_out;

    char* ws = (char*)d_ws;
    size_t off = 0;
    auto take = [&](size_t bytes) {
        char* p = ws + off;
        off += (bytes + 255) & ~(size_t)255;
        return p;
    };
    bf16* xn = (bf16*)take((size_t)SEQ * DM * 2);
    bf16* h = (bf16*)take((size_t)SEQ * DFF * 2);
    cf* Z1 = (cf*)take((size_t)SEQ * KP * 8);
    cf* Z2 = (cf*)take((size_t)SEQ * KP * 8);
    float* p0 = (float*)take((size_t)SEQ * DM * 4);
    float* p1 = (float*)take((size_t)SEQ * DM * 4);

    (void)hipFuncSetAttribute((const void*)k_gemm1, hipFuncAttributeMaxDynamicSharedMemorySize, G1_SHM);
    (void)hipFuncSetAttribute((const void*)k_gemm2, hipFuncAttributeMaxDynamicSharedMemorySize, G2_SHM);

    k_ln_fft_fwd<<<256, 576, 0, stream>>>(x, xn, Z1);
    k_fft_col<<<8 * 33, 256, 0, stream>>>(Z1, Z2, wre, wim);
    k_gemm1<<<224, 512, G1_SHM, stream>>>(xn, wg, wu, h, Z1, Z2, wre, wim);
    k_gemm2<<<256, 512, G2_SHM, stream>>>(h, wd, p0, p1);
    k_fft_inv_out<<<256, 576, 0, stream>>>(Z2, x, p0, p1, out);
}
```

```cpp
#include <hip/hip_runtime.h>
#include <hip/hip_bf16.h>
#include <stdint.h>

typedef __hip_bfloat16 bf16;
typedef short bf16x8 __attribute__((ext_vector_type(8)));
typedef short bf16x4 __attribute__((ext_vector_type(4)));
typedef float f32x4 __attribute__((ext_vector_type(4)));

#define DEV __device__ __forceinline__

constexpr int SEQ = 2048;
constexpr int DM = 1024;
constexpr int KF = 513;
constexpr int KP = 520;
constexpr int DFF = 3584;

struct cf { float x, y; };

DEV cf cadd(cf a, cf b) { return {a.x + b.x, a.y + b.y}; }
DEV cf csub(cf a, cf b) { return {a.x - b.x, a.y - b.y}; }
DEV cf cmul(cf a, cf b) { return {a.x * b.x - a.y * b.y, a.x * b.y + a.y * b.x}; }
DEV cf cmulc(cf a, cf b) { return {a.x * b.x + a.y * b.y, a.y * b.x - a.x * b.y}; }
DEV cf cconj(cf a) { return {a.x, -a.y}; }

DEV float bfr(float x) {
    unsigned u = __float_as_uint(x);
    u += 0x7fffu + ((u >> 16) & 1u);
    return __uint_as_float(u & 0xffff0000u);
}
DEV unsigned short bf_bits(float x) {
    unsigned u = __float_as_uint(x);
    u += 0x7fffu + ((u >> 16) & 1u);
    return (unsigned short)(u >> 16);
}
DEV float bf_lo(unsigned v) { return __uint_as_float(v << 16); }
DEV float bf_hi(unsigned v) { return __uint_as_float(v & 0xffff0000u); }
DEV float bf2f(bf16 v) { return __uint_as_float(((unsigned)*reinterpret_cast<const unsigned short*>(&v)) << 16); }

DEV constexpr float c16(int j) {
    return j == 0 ? 1.0f : j == 1 ? 0.92387953251128674f : j == 2 ? 0.70710678118654752f : j == 3 ? 0.38268343236508977f
         : j == 4 ? 0.0f : j == 5 ? -0.38268343236508977f : j == 6 ? -0.70710678118654752f : -0.92387953251128674f;
}
DEV constexpr float s16(int j) {
    return j == 0 ? 0.0f : j == 1 ? 0.38268343236508977f : j == 2 ? 0.70710678118654752f : j == 3 ? 0.92387953251128674f
         : j == 4 ? 1.0f : j == 5 ? 0.92387953251128674f : j == 6 ? 0.70710678118654752f : 0.38268343236508977f;
}

template <int N, int SG>
struct Dft {
    static DEV void run(cf* v) {
        cf a[N / 2], b[N / 2];
#pragma unroll
        for (int j = 0; j < N / 2; ++j) {
            a[j] = cadd(v[j], v[j + N / 2]);
            cf d = csub(v[j], v[j + N / 2]);
            const int t = j * (16 / N);
            if (t == 0) {
                b[j] = d;
            } else if (t == 4) {
                b[j] = SG < 0 ? cf{d.y, -d.x} : cf{-d.y, d.x};
            } else {
                const float cr = c16(t), si = SG * s16(t);
                b[j] = {d.x * cr - d.y * si, d.x * si + d.y * cr};
            }
        }
        Dft<N / 2, SG>::run(a);
        Dft<N / 2, SG>::run(b);
#pragma unroll
        for (int r = 0; r < N / 2; ++r) {
            v[2 * r] = a[r];
            v[2 * r + 1] = b[r];
        }
    }
};
template <int SG>
struct Dft<1, SG> {
    static DEV void run(cf*) {}
};

DEV void build_tw2048(cf* tw, int tid, int nthreads) {
    for (int j = tid; j < 2048; j += nthreads) {
        float s, c;
        sincospif((float)j * (1.0f / 1024.0f), &s, &c);
        tw[j] = {c, -s};
    }
}
template <int SG>
DEV cf twv(const cf* tw, int j) {
    cf t = tw[j];
    return SG < 0 ? t : cf{t.x, -t.y};
}

constexpr int RLEN = 576;

template <int SG>
DEV void fft512_wave(cf (&v)[8], cf* ex, const cf* tw, int lane) {
    Dft<8, SG>::run(v);
#pragma unroll
    for (int r = 1; r < 8; ++r) v[r] = cmul(v[r], twv<SG>(tw, 4 * lane * r));
#pragma unroll
    for (int r = 0; r < 8; ++r) ex[r * 68 + lane] = v[r];
    __builtin_amdgcn_wave_barrier();
    const int l0 = lane & 7, rr = lane >> 3;
#pragma unroll
    for (int l1 = 0; l1 < 8; ++l1) v[l1] = ex[rr * 68 + l0 + 8 * l1];
    __builtin_amdgcn_wave_barrier();
    Dft<8, SG>::run(v);
#pragma unroll
    for (int q1 = 1; q1 < 8; ++q1) v[q1] = cmul(v[q1], twv<SG>(tw, 32 * l0 * q1));
#pragma unroll
    for (int q1 = 0; q1 < 8; ++q1) ex[q1 * 66 + lane] = v[q1];
    __builtin_amdgcn_wave_barrier();
    const int r2 = lane & 7, q1s = lane >> 3;
#pragma unroll
    for (int l0i = 0; l0i < 8; ++l0i) v[l0i] = ex[q1s * 66 + l0i + 8 * r2];
    __builtin_amdgcn_wave_barrier();
    Dft<8, SG>::run(v);
}

__global__ __launch_bounds__(512) void k_ln_fft_fwd(const bf16* __restrict__ x, bf16* __restrict__ xn,
                                                    cf* __restrict__ Z1) {
    __shared__ cf tw[2048];
    __shared__ cf R[8][RLEN];
    const int tid = threadIdx.x, lane = tid & 63, b = tid >> 6;
    const int a = blockIdx.x;
    const int row = a + 256 * b;

    const unsigned* xr = reinterpret_cast<const unsigned*>(x + (size_t)row * DM);
    unsigned raw[8];
#pragma unroll
    for (int j = 0; j < 8; ++j) raw[j] = xr[lane + 64 * j];

    build_tw2048(tw, tid, 512);

    float sum = 0.f;
#pragma unroll
    for (int j = 0; j < 8; ++j) sum += bf_lo(raw[j]) + bf_hi(raw[j]);
#pragma unroll
    for (int o = 32; o > 0; o >>= 1) sum += __shfl_xor(sum, o);
    const float mean = sum * (1.0f / DM);
    float sq = 0.f;
#pragma unroll
    for (int j = 0; j < 8; ++j) {
        float d0 = bf_lo(raw[j]) - mean, d1 = bf_hi(raw[j]) - mean;
        sq += d0 * d0 + d1 * d1;
    }
#pragma unroll
    for (int o = 32; o > 0; o >>= 1) sq += __shfl_xor(sq, o);
    const float mb = bfr(mean);
    const float vb = bfr(sq * (1.0f / DM));
    const float den = bfr(sqrtf(bfr(vb + 1e-5f)));

    cf v[8];
    unsigned* xo = reinterpret_cast<unsigned*>(xn + (size_t)row * DM);
#pragma unroll
    for (int j = 0; j < 8; ++j) {
        float e0 = bfr(bfr(bf_lo(raw[j]) - mb) / den);
        float e1 = bfr(bfr(bf_hi(raw[j]) - mb) / den);
        v[j] = {e0, e1};
        xo[lane + 64 * j] = (__float_as_uint(e0) >> 16) | (__float_as_uint(e1) & 0xffff0000u);
    }
    __syncthreads();

    fft512_wave<-1>(v, R[b], tw, lane);
#pragma unroll
    for (int j = 0; j < 8; ++j) R[b][lane + 64 * j] = v[j];
    __syncthreads();

    for (int k = tid; k < KF; k += 512) {
        const int k1 = k & 511, k2 = (512 - k) & 511;
        const cf w = tw[2 * k];
        cf u[8];
#pragma unroll
        for (int bb = 0; bb < 8; ++bb) {
            cf p = R[bb][k1], q = cconj(R[bb][k2]);
            cf e = {0.5f * (p.x + q.x), 0.5f * (p.y + q.y)};
            cf o = {0.5f * (p.x - q.x), 0.5f * (p.y - q.y)};
            cf wo = cmul(w, o);
            u[bb] = {e.x + wo.y, e.y - wo.x};
        }
        Dft<8, -1>::run(u);
#pragma unroll
        for (int bp = 0; bp < 8; ++bp) {
            cf o = bp ? cmul(u[bp], tw[a * bp]) : u[bp];
            Z1[(size_t)(a * 8 + bp) * KP + k] = o;
        }
    }
}

constexpr int CT_LD = 17;
DEV void fft_col_tile(const cf* __restrict__ Z1, cf* __restrict__ Z2, const bf16* __restrict__ wr,
                      const bf16* __restrict__ wi, cf* T, const cf* tw, int bp, int kt, int t) {
    const int k0 = kt * 16;
    const int kk = t & 15, j = t >> 4;
    const int kcl = (k0 + kk < KF) ? (k0 + kk) : 0;
    unsigned short wq0[16], wq1[16];
#pragma unroll
    for (int c = 0; c < 16; ++c) {
        const int sp = 8 * (j + 16 * c) + bp;
        wq0[c] = *reinterpret_cast<const unsigned short*>(wr + (size_t)sp * KF + kcl);
        wq1[c] = *reinterpret_cast<const unsigned short*>(wi + (size_t)sp * KF + kcl);
    }
#pragma unroll
    for (int i = 0; i < 8; ++i) {
        const int a = (t >> 3) + 32 * i, c = t & 7;
        float4 g = float4{0.f, 0.f, 0.f, 0.f};
        if (k0 + 2 * c < KP) g = *reinterpret_cast<const float4*>(Z1 + (size_t)(a * 8 + bp) * KP + k0 + 2 * c);
        T[a * CT_LD + 2 * c] = {g.x, g.y};
        T[a * CT_LD + 2 * c + 1] = {g.z, g.w};
    }
    __syncthreads();
    cf v[16];
#pragma unroll
    for (int i = 0; i < 16; ++i) v[i] = T[(j + 16 * i) * CT_LD + kk];
    Dft<16, -1>::run(v);
#pragma unroll
    for (int d = 1; d < 16; ++d) v[d] = cmul(v[d], tw[j * d]);
#pragma unroll
    for (int d = 0; d < 16; ++d) T[(j + 16 * d) * CT_LD + kk] = v[d];
    __syncthreads();
    const int dd = j;
#pragma unroll
    for (int i = 0; i < 16; ++i) v[i] = T[(i + 16 * dd) * CT_LD + kk];
    Dft<16, -1>::run(v);
#pragma unroll
    for (int c = 0; c < 16; ++c)
        v[c] = cmul(v[c], cf{__uint_as_float((unsigned)wq0[c] << 16), __uint_as_float((unsigned)wq1[c] << 16)});
    Dft<16, 1>::run(v);
#pragma unroll
    for (int i = 1; i < 16; ++i) v[i] = cmulc(v[i], tw[i * dd]);
#pragma unroll
    for (int i = 0; i < 16; ++i) T[(i + 16 * dd) * CT_LD + kk] = v[i];
    __syncthreads();
#pragma unroll
    for (int d = 0; d < 16; ++d) v[d] = T[(j + 16 * d) * CT_LD + kk];
    Dft<16, 1>::run(v);
#pragma unroll
    for (int i = 0; i < 16; ++i) T[(j + 16 * i) * CT_LD + kk] = v[i];
    __syncthreads();
#pragma unroll
    for (int i = 0; i < 8; ++i) {
        const int a = (t >> 3) + 32 * i, c = t & 7;
        const cf p = T[a * CT_LD + 2 * c], q = T[a * CT_LD + 2 * c + 1];
        if (k0 + 2 * c < KP)
            *reinterpret_cast<float4*>(Z2 + (size_t)(a * 8 + bp) * KP + k0 + 2 * c) = float4{p.x, p.y, q.x, q.y};
    }
    __syncthreads();
}

__global__ __launch_bounds__(256) void k_fft_col(const cf* __restrict__ Z1, cf* __restrict__ Z2,
                                                 const bf16* __restrict__ wr, const bf16* __restrict__ wi) {
    __shared__ cf T[256 * CT_LD];
    __shared__ cf tw[256];
    const int t = threadIdx.x;
    {
        float s, c;
        sincospif((float)t * (1.0f / 128.0f), &s, &c);
        tw[t] = {c, -s};
    }
    const int bp = blockIdx.x & 7, kt = blockIdx.x >> 3;
    fft_col_tile(Z1, Z2, wr, wi, T, tw, bp, kt, t);
}

__global__ __launch_bounds__(512) void k_fft_inv_out(const cf* __restrict__ Z2, const bf16* __restrict__ x,
                                                     const float* __restrict__ p0, const float* __restrict__ p1,
                                                     bf16* __restrict__ out) {
    __shared__ cf tw[2048];
    __shared__ cf R[8][RLEN];
    const int tid = threadIdx.x, lane = tid & 63, b = tid >> 6;
    const int a = blockIdx.x;
    build_tw2048(tw, tid, 512);
    __syncthreads();
    for (int k = tid; k < KF; k += 512) {
        cf u[8];
#pragma unroll
        for (int bp = 0; bp < 8; ++bp) {
            cf z = Z2[(size_t)(a * 8 + bp) * KP + k];
            u[bp] = bp ? cmulc(z, tw[a * bp]) : z;
        }
        Dft<8, 1>::run(u);
#pragma unroll
        for (int bb = 0; bb < 8; ++bb) R[bb][k] = u[bb];
    }
    __syncthreads();

    cf v[8];
#pragma unroll
    for (int j = 0; j < 8; ++j) {
        const int k = lane + 64 * j;
        cf p = R[b][k], q = R[b][512 - k];
        if (k == 0) { p.y = 0.f; q.y = 0.f; }
        q = cconj(q);
        cf e = cadd(p, q);
        cf o = cmulc(csub(p, q), tw[2 * k]);
        v[j] = {e.x - o.y, e.y + o.x};
    }
    __builtin_amdgcn_wave_barrier();
    fft512_wave<1>(v, R[b], tw, lane);

    const int row = a + 256 * b;
    const unsigned* xr = reinterpret_cast<const unsigned*>(x + (size_t)row * DM);
    const float2* q0 = reinterpret_cast<const float2*>(p0 + (size_t)row * DM);
    const float2* q1 = reinterpret_cast<const float2*>(p1 + (size_t)row * DM);
    unsigned* orow = reinterpret_cast<unsigned*>(out + (size_t)row * DM);
    const float sc = 1.0f / (2048.0f * 1024.0f);
#pragma unroll
    for (int j = 0; j < 8; ++j) {
        const int n = lane + 64 * j;
        const unsigned xv = xr[n];
        const float2 a0 = q0[n], a1 = q1[n];
        const float d0 = bfr(a0.x + a1.x), d1 = bfr(a0.y + a1.y);
        const float g0 = bfr(d0 / (1.0f + __expf(-d0))), g1 = bfr(d1 / (1.0f + __expf(-d1)));
        const float s0 = bfr(v[j].x * sc), s1 = bfr(v[j].y * sc);
        const float o0 = bfr(bf_lo(xv) + bfr(s0 * g0)), o1 = bfr(bf_hi(xv) + bfr(s1 * g1));
        orow[n] = (__float_as_uint(o0) >> 16) | (__float_as_uint(o1) & 0xffff0000u);
    }
}

DEV int lds_byte(int r, int c) {
    int st = (r >> 4) * 2 + (c >> 5), rr = r & 15, cc = c & 31, ob = rr * 64 + cc * 2;
    return st * 1024 + (ob ^ (((ob >> 9) & 1) << 5));
}
DEV void stage_rc(int b, int& R, int& C) {
    int st = b / 1024, sb = b % 1024, swz = sb ^ (((sb >> 9) & 1) << 5);
    R = (st >> 1) * 16 + swz / 64;
    C = (st & 1) * 32 + (swz % 64) / 2;
}
DEV int b_off(int row, int ch) { return 256 * row + 16 * (ch ^ (((row & 3) << 2) | ((row >> 2) & 3))); }

DEV unsigned tr_off(int c, int ks, int t, int lane) {
    const int g = lane >> 4, q = (lane & 15) >> 2, p = lane & 3;
    return (unsigned)(b_off(32 * ks + 8 * g + 4 * t + q, 2 * c + (p >> 1)) + 8 * (p & 1));
}
#define TR_READ(dst, addr, OFF) \
    asm volatile("ds_read_b64_tr_b16 %0, %1 offset:%2" : "=v"(dst) : "v"(addr), "i"(OFF))
#define PIN(x) asm volatile("" : "+v"(x))
DEV bf16x8 cat8(bf16x4 lo, bf16x4 hi) {
    bf16x8 r;
    r[0] = lo[0]; r[1] = lo[1]; r[2] = lo[2]; r[3] = lo[3];
    r[4] = hi[0]; r[5] = hi[1]; r[6] = hi[2]; r[7] = hi[3];
    return r;
}

DEV float gelu_tanh(float x) {
    const float u2 = 1.5957691216057308f * (x + 0.044715f * x * x * x);
    return x * __frcp_rn(1.0f + __expf(-u2));
}
DEV unsigned pack_bf16(float a, float b);
DEV unsigned geglu_pair(float g0, float g1, float u0, float u1) {
    const unsigned gp = pack_bf16(g0, g1), up = pack_bf16(u0, u1);
    const float a0 = bf_lo(gp), a1 = bf_hi(gp);
    const float c1 = -1.5957691216057308f * 1.4426950408889634f, c3 = c1 * 0.044715f;
    const float e0 = __builtin_amdgcn_exp2f(a0 * fmaf(a0 * a0, c3, c1));
    const float e1 = __builtin_amdgcn_exp2f(a1 * fmaf(a1 * a1, c3, c1));
    const float t0 = a0 * __builtin_amdgcn_rcpf(1.0f + e0), t1 = a1 * __builtin_amdgcn_rcpf(1.0f + e1);
    const unsigned tp = pack_bf16(t0, t1);
    return pack_bf16(bf_lo(tp) * bf_lo(up), bf_hi(tp) * bf_hi(up));
}
DEV unsigned pack_bf16(float a, float b) {
    unsigned r;
    asm("v_cvt_pk_bf16_f32 %0, %1, %2" : "=v"(r) : "v"(a), "v"(b));
    return r;
}

#define WAIT_V(n) asm volatile("s_waitcnt vmcnt(" #n ")" ::: "memory")
#define WAIT_L(n) asm volatile("s_waitcnt lgkmcnt(" #n ")" ::: "memory")
#define BAR __builtin_amdgcn_s_barrier()
#define SCHED __builtin_amdgcn_sched_barrier(0)

constexpr int G1_HT = 128 * 64;
constexpr int G1_SHM = 8 * G1_HT * 2;

__global__ __launch_bounds__(512, 2) void k_gemm1(const bf16* __restrict__ A, const bf16* __restrict__ Wg,
                                                  const bf16* __restrict__ Wu, bf16* __restrict__ H,
                                                  const cf* __restrict__ Z1, cf* __restrict__ Z2,
                                                  const bf16* __restrict__ wre, const bf16* __restrict__ wim) {
    extern __shared__ __attribute__((aligned(16))) bf16 shm[];
    if (blockIdx.x >= 224) {
        cf* base = reinterpret_cast<cf*>(shm);
        cf* tw = base + 2 * 256 * CT_LD;
        const int tid = threadIdx.x, half = tid >> 8, t = tid & 255;
        if (tid < 256) {
            float sn, cs;
            sincospif((float)tid * (1.0f / 128.0f), &sn, &cs);
            tw[tid] = {cs, -sn};
        }
        for (int pr = blockIdx.x - 224; pr < 132; pr += 32) {
            const int tile = pr * 2 + half;
            fft_col_tile(Z1, Z2, wre, wim, base + half * 256 * CT_LD, tw, tile & 7, tile >> 3, t);
        }
        return;
    }
    constexpr int K = DM, LDB = DFF;
#define SA(b, h) (shm + (4 + (b) * 2 + (h)) * G1_HT)
#define SB(b, h) (shm + ((b) * 2 + (h)) * G1_HT)
#define STAGE_A_X(T, P, br, kt)                                                                   \
    do {                                                                                          \
        const bf16* _g = A + (size_t)(br) * K + (kt) * 64;                                        \
        for (int _i = 0; _i < 2; ++_i) {                                                          \
            int _b = (T) * 16 + _i * 8192, _r, _c;                                                \
            stage_rc(_b, _r, _c);                                                                 \
            __builtin_amdgcn_global_load_lds((const unsigned*)(_g + (size_t)_r * K + _c),         \
                                             (unsigned*)((char*)(P) + _b), 16, 0, 0);             \
        }                                                                                         \
    } while (0)
#define STAGE_A(P, br, kt) STAGE_A_X(threadIdx.x, P, br, kt)
#define STAGE_B(P, W, kt)                                                                         \
    do {                                                                                          \
        const bf16* _g = (W) + (size_t)(kt) * 64 * LDB + ncol0;                                   \
        for (int _i = 0; _i < 2; ++_i) {                                                          \
            int _b = threadIdx.x * 16 + _i * 8192, _r = _b >> 8, _p = (_b >> 4) & 15;             \
            int _ch = _p ^ (((_r & 3) << 2) | ((_r >> 2) & 3));                                   \
            __builtin_amdgcn_global_load_lds((const unsigned*)(_g + (size_t)_r * LDB + _ch * 8),  \
                                             (unsigned*)((char*)(P) + _b), 16, 0, 0);             \
        }                                                                                         \
    } while (0)
#define LDA(dst, b, h)                                                                            \
    for (int m = 0; m < 4; ++m)                                                                   \
        for (int k = 0; k < 2; ++k)                                                               \
    dst[m][k] = *reinterpret_cast<const bf16x8*>((char*)SA(b, h) + lds_byte(wr * 64 + m * 16 + fr, k * 32 + fq * 8))
#define LDB_(dst, b, h)                                                                           \
    for (int n = 0; n < 2; ++n)                                                                   \
        for (int k = 0; k < 2; ++k) {                                                             \
            TR_READ(dst##_lo[n][k], tro[n][k][0], ((b) * 2 + (h)) * 16384);                         \
            TR_READ(dst##_hi[n][k], tro[n][k][1], ((b) * 2 + (h)) * 16384);                         \
        }
#define FIXB(dst)                                                                                 \
    for (int n = 0; n < 2; ++n)                                                                   \
        for (int k = 0; k < 2; ++k) {                                                             \
            PIN(dst##_lo[n][k]); PIN(dst##_hi[n][k]);                                                 \
            dst[n][k] = cat8(dst##_lo[n][k], dst##_hi[n][k]);                                         \
        }
#define MMA(ai, bj, At, Bt)                                                                       \
    do {                                                                                          \
        __builtin_amdgcn_s_setprio(1);                                                            \
        for (int m = 0; m < 4; ++m)                                                               \
            for (int n = 0; n < 2; ++n)                                                           \
                for (int k = 0; k < 2; ++k)                                                       \
                    acc[ai][bj][m][n] =                                                           \
                        __builtin_amdgcn_mfma_f32_16x16x32_bf16(Bt[n][k], At[m][k], acc[ai][bj][m][n], 0, 0, 0); \
        __builtin_amdgcn_s_setprio(0);                                                            \
    } while (0)

    const int nM = SEQ / 256;
    const int wgid = (blockIdx.x & 7) * 28 + (blockIdx.x >> 3);
    const int pm = wgid % nM, pn = wgid / nM;
    const int brow = pm * 256, ncol0 = pn * 128;
    int wid = threadIdx.x >> 6, lane = threadIdx.x & 63, wr = wid >> 2, wc = wid & 3, fr = lane & 15, fq = lane >> 4;
    f32x4 acc[2][2][4][2] = {};
    bf16x8 At[4][2], B0[2][2], B1[2][2];
    bf16x4 B0_lo[2][2], B0_hi[2][2], B1_lo[2][2], B1_hi[2][2];
    unsigned tro[2][2][2];
    for (int n = 0; n < 2; ++n)
        for (int k = 0; k < 2; ++k)
            for (int t = 0; t < 2; ++t) tro[n][k][t] = (unsigned)(size_t)shm + tr_off(wc * 2 + n, k, t, lane);
    constexpr int nt = K / 64;

    STAGE_B(SB(0, 0), Wg, 0); STAGE_A(SA(0, 0), brow, 0);
    STAGE_B(SB(0, 1), Wu, 0); STAGE_A(SA(0, 1), brow + 128, 0);
    if (wr == 1) BAR;
    WAIT_V(4); BAR;
    STAGE_B(SB(1, 0), Wg, 1); STAGE_A(SA(1, 0), brow, 1); STAGE_B(SB(1, 1), Wu, 1);
    WAIT_V(6); BAR;
    for (int t = 0; t < nt - 2; t += 2) {
        LDB_(B0, 0, 0); SCHED; LDA(At, 0, 0); STAGE_A(SA(1, 1), brow + 128, t + 1);
        WAIT_L(8); BAR; WAIT_L(0); FIXB(B0); MMA(0, 0, At, B0); BAR; SCHED;
        LDB_(B1, 0, 1); STAGE_B(SB(0, 0), Wg, t + 2);
        BAR; WAIT_L(0); FIXB(B1); MMA(0, 1, At, B1); BAR;
        LDA(At, 0, 1); STAGE_A(SA(0, 0), brow, t + 2);
        BAR; WAIT_L(0); MMA(1, 0, At, B0); BAR; SCHED;
        STAGE_B(SB(0, 1), Wu, t + 2);
        WAIT_V(6); BAR; MMA(1, 1, At, B1); BAR;
        LDB_(B0, 1, 0); SCHED; LDA(At, 1, 0); STAGE_A(SA(0, 1), brow + 128, t + 2);
        WAIT_L(8); BAR; WAIT_L(0); FIXB(B0); MMA(0, 0, At, B0); BAR; SCHED;
        LDB_(B1, 1, 1); STAGE_B(SB(1, 0), Wg, t + 3);
        BAR; WAIT_L(0); FIXB(B1); MMA(0, 1, At, B1); BAR;
        LDA(At, 1, 1); STAGE_A(SA(1, 0), brow, t + 3);
        BAR; WAIT_L(0); MMA(1, 0, At, B0); BAR; SCHED;
        STAGE_B(SB(1, 1), Wu, t + 3);
        WAIT_V(6); BAR; MMA(1, 1, At, B1); BAR;
    }
    int tz = threadIdx.x;
    asm volatile("" : "+v"(tz));
    wid = tz >> 6; lane = tz & 63; wr = wid >> 2; wc = wid & 3; fr = lane & 15; fq = lane >> 4;
    {
        LDB_(B0, 0, 0); WAIT_V(0); LDA(At, 0, 0); STAGE_A_X(tz, SA(1, 1), brow + 128, nt - 1);
        BAR; WAIT_L(0); FIXB(B0); MMA(0, 0, At, B0); BAR;
        LDB_(B1, 0, 1); BAR; WAIT_L(0); FIXB(B1); MMA(0, 1, At, B1); BAR;
        LDA(At, 0, 1); WAIT_V(4); BAR; WAIT_L(0); MMA(1, 0, At, B0); MMA(1, 1, At, B1); BAR;
    }
    {
        LDB_(B0, 1, 0); LDA(At, 1, 0); WAIT_V(2); BAR; WAIT_L(0); FIXB(B0); MMA(0, 0, At, B0); BAR;
        LDB_(B1, 1, 1); WAIT_V(0); BAR; WAIT_L(0); FIXB(B1); MMA(0, 1, At, B1); BAR;
        LDA(At, 1, 1); BAR; WAIT_L(0); MMA(1, 0, At, B0); MMA(1, 1, At, B1); BAR;
    }
    if (wr == 0) BAR;
    {
        char* tile = reinterpret_cast<char*>(shm);
#pragma unroll
        for (int ai = 0; ai < 2; ++ai)
#pragma unroll
            for (int m = 0; m < 4; ++m)
#pragma unroll
                for (int n = 0; n < 2; ++n) {
                    const int row = ai * 128 + wr * 64 + m * 16 + fr;
                    const int col = wc * 32 + n * 16 + fq * 4;
                    uint2 pk;
                    pk.x = geglu_pair(acc[ai][0][m][n][0], acc[ai][0][m][n][1], acc[ai][1][m][n][0], acc[ai][1][m][n][1]);
                    pk.y = geglu_pair(acc[ai][0][m][n][2], acc[ai][0][m][n][3], acc[ai][1][m][n][2], acc[ai][1][m][n][3]);
                    *reinterpret_cast<uint2*>(tile + row * 272 + col * 2) = pk;
                }
        __syncthreads();
#pragma unroll
        for (int i = 0; i < 8; ++i) {
            const int row = wid * 32 + i * 4 + (lane >> 4), c16 = lane & 15;
            const uint4 v = *reinterpret_cast<const uint4*>(tile + row * 272 + c16 * 16);
            *reinterpret_cast<uint4*>(H + (size_t)(brow + row) * DFF + ncol0 + c16 * 8) = v;
        }
    }
#undef SA
#undef SB
#undef STAGE_A_X
#undef STAGE_A
#undef STAGE_B
#undef LDA
#undef LDB_
#undef FIXB
#undef MMA
}

constexpr int G2_SHM = 4 * 32768;

__global__ __launch_bounds__(512, 2) void k_gemm2(const bf16* __restrict__ A, const bf16* __restrict__ W,
                                                  float* __restrict__ P0, float* __restrict__ P1) {
    extern __shared__ __attribute__((aligned(16))) char lds2[];
    constexpr int K = DFF, LDB = DM, KH = K / 2;
    const int wgid = blockIdx.x;
    const int sk = wgid & 1, tile = wgid >> 1;
    const int pm = tile & 15, pn = tile >> 4;
    const int brow = pm * 128, ncol0 = pn * 128, kbase = sk * KH;
    int tid = threadIdx.x, wid = tid >> 6, lane = tid & 63, wr = wid >> 2, wc = wid & 3, fr = lane & 15,
        fq = lane >> 4;
    f32x4 acc[4][2] = {};
    constexpr int nt = KH / 64;

    int ar[2], ac[2], brr[2], bch[2];
#pragma unroll
    for (int i = 0; i < 2; ++i) {
        int b = tid * 16 + i * 8192;
        stage_rc(b, ar[i], ac[i]);
        brr[i] = b >> 8;
        bch[i] = ((b >> 4) & 15) ^ (((brr[i] & 3) << 2) | ((brr[i] >> 2) & 3));
    }
    const bf16* ga = A + (size_t)brow * K + kbase;
    const bf16* gb = W + (size_t)kbase * LDB + ncol0;
#define G2_STAGE(buf, kt)                                                                              \
    do {                                                                                               \
        _Pragma("unroll") for (int i = 0; i < 2; ++i)                                                  \
            __builtin_amdgcn_global_load_lds((const unsigned*)(gb + (size_t)((kt) * 64 + brr[i]) * LDB + bch[i] * 8), \
                                             (unsigned*)(lds2 + (buf) * 16384 + tid * 16 + i * 8192), 16, 0, 0); \
        _Pragma("unroll") for (int i = 0; i < 2; ++i)                                                  \
            __builtin_amdgcn_global_load_lds((const unsigned*)(ga + (size_t)ar[i] * K + (kt) * 64 + ac[i]), \
                                             (unsigned*)(lds2 + 65536 + (buf) * 16384 + tid * 16 + i * 8192), 16, 0, 0); \
    } while (0)

    unsigned tro[2][2][2];
#pragma unroll
    for (int n = 0; n < 2; ++n)
#pragma unroll
        for (int k = 0; k < 2; ++k)
#pragma unroll
            for (int t = 0; t < 2; ++t) tro[n][k][t] = (unsigned)(size_t)lds2 + tr_off(wc * 2 + n, k, t, lane);

    bf16x8 At[4][2], Bt[2][2];
    bf16x4 Bl[2][2], Bh[2][2];
#define G2_STEP(buf, t, DO_STAGE, VW)                                                                  \
    do {                                                                                               \
        _Pragma("unroll") for (int n = 0; n < 2; ++n) _Pragma("unroll") for (int k = 0; k < 2; ++k) {  \
            TR_READ(Bl[n][k], tro[n][k][0], (buf) * 16384);                                            \
            TR_READ(Bh[n][k], tro[n][k][1], (buf) * 16384);                                            \
        }                                                                                              \
        _Pragma("unroll") for (int m = 0; m < 4; ++m) _Pragma("unroll") for (int k = 0; k < 2; ++k)    \
            At[m][k] = *reinterpret_cast<const bf16x8*>(lds2 + 65536 + (buf) * 16384 +                 \
                                                        lds_byte(wr * 64 + m * 16 + fr, k * 32 + fq * 8)); \
        if (DO_STAGE) G2_STAGE(((buf) + 3) & 3, (t) + 3);                                              \
        WAIT_L(0);                                                                                     \
        BAR;                                                                                           \
        _Pragma("unroll") for (int n = 0; n < 2; ++n) _Pragma("unroll") for (int k = 0; k < 2; ++k) {  \
            PIN(Bl[n][k]); PIN(Bh[n][k]);                                                              \
            Bt[n][k] = cat8(Bl[n][k], Bh[n][k]);                                                       \
        }                                                                                              \
        __builtin_amdgcn_s_setprio(1);                                                                 \
        _Pragma("unroll") for (int m = 0; m < 4; ++m) _Pragma("unroll") for (int n = 0; n < 2; ++n)    \
            _Pragma("unroll") for (int k = 0; k < 2; ++k)                                              \
                acc[m][n] = __builtin_amdgcn_mfma_f32_16x16x32_bf16(Bt[n][k], At[m][k], acc[m][n], 0, 0, 0); \
        __builtin_amdgcn_s_setprio(0);                                                                 \
        WAIT_V(VW);                                                                                    \
        BAR;                                                                                           \
    } while (0)

    G2_STAGE(0, 0);
    G2_STAGE(1, 1);
    G2_STAGE(2, 2);
    if (wr == 1) BAR;
    WAIT_V(4);
    BAR;
    BAR;
    for (int t = 0; t < nt - 4; t += 4) {
        G2_STEP(0, t, true, 4);
        G2_STEP(1, t + 1, true, 4);
        G2_STEP(2, t + 2, true, 4);
        G2_STEP(3, t + 3, true, 4);
    }
    G2_STEP(0, nt - 4, true, 4);
    G2_STEP(1, nt - 3, false, 0);
    G2_STEP(2, nt - 2, false, 0);
    G2_STEP(3, nt - 1, false, 0);
    if (wr == 0) BAR;
    float* P = sk ? P1 : P0;
#pragma unroll
    for (int m = 0; m < 4; ++m)
#pragma unroll
        for (int n = 0; n < 2; ++n) {
            const int row = brow + wr * 64 + m * 16 + fr;
            const int col = ncol0 + wc * 32 + n * 16 + fq * 4;
            *reinterpret_cast<f32x4*>(P + (size_t)row * DM + col) = acc[m][n];
        }
#undef G2_STAGE
#undef G2_STEP
}

extern "C" void kernel_launch(void* const* d_in, const int* in_sizes, int n_in, void* d_out, int out_size,
                              void* d_ws, size_t ws_size, hipStream_t stream) {
    const bf16* x = (const bf16*)d_in[0];
    const bf16* wre = (const bf16*)d_in[1];
    const bf16* wim = (const bf16*)d_in[2];
    const bf16* wg = (const bf16*)d_in[4];
    const bf16* wu = (const bf16*)d_in[5];
    const bf16* wd = (const bf16*)d_in[6];
    bf16* out = (bf16*)d_out;

    char* ws = (char*)d_ws;
    size_t off = 0;
    auto take = [&](size_t bytes) {
        char* p = ws + off;
        off += (bytes + 255) & ~(size_t)255;
        return p;
    };
    bf16* xn = (bf16*)take((size_t)SEQ * DM * 2);
    bf16* h = (bf16*)take((size_t)SEQ * DFF * 2);
    cf* Z1 = (cf*)take((size_t)SEQ * KP * 8);
    cf* Z2 = (cf*)take((size_t)SEQ * KP * 8);
    float* p0 = (float*)take((size_t)SEQ * DM * 4);
    float* p1 = (float*)take((size_t)SEQ * DM * 4);

    (void)hipFuncSetAttribute((const void*)k_gemm1, hipFuncAttributeMaxDynamicSharedMemorySize, G1_SHM);
    (void)hipFuncSetAttribute((const void*)k_gemm2, hipFuncAttributeMaxDynamicSharedMemorySize, G2_SHM);

    k_ln_fft_fwd<<<256, 512, 0, stream>>>(x, xn, Z1);
    k_fft_col<<<8 * 33, 256, 0, stream>>>(Z1, Z2, wre, wim);
    k_gemm1<<<224, 512, G1_SHM, stream>>>(xn, wg, wu, h, Z1, Z2, wre, wim);
    k_gemm2<<<256, 512, G2_SHM, stream>>>(h, wd, p0, p1);
    k_fft_inv_out<<<256, 512, 0, stream>>>(Z2, x, p0, p1, out);
}
```
